```python
import torch

def _softmax(x, axis=-1):
    return torch.softmax(x, dim=axis)
def _gelu(x):
    return torch.nn.functional.gelu(x, approximate="tanh")
def _silu(x):
    return torch.nn.functional.silu(x)
def _sigmoid(x):
    return torch.sigmoid(x)
def _layernorm(x, axis=-1, eps=1e-5):
    m = x.mean(dim=axis, keepdim=True)
    v = x.var(dim=axis, keepdim=True, unbiased=False)
    return (x - m) / torch.sqrt(v + eps)
def _scatter_add(tgt, idx, src, axis=0):
    return tgt.clone().index_add_(axis, idx, src)
def _segment_sum(src, seg, n):
    out = torch.zeros((n,) + src.shape[1:], dtype=src.dtype)
    return out.index_add_(0, seg, src)
def _pad(x, pw):
    flat = tuple(v for pair in reversed(pw) for v in pair)
    return torch.nn.functional.pad(x, flat)

kernel_name = 'standalone__gqa_softmax__near_ridge'

def setup_inputs():

    _w = (lambda a: (lambda t: t.to(torch.bfloat16) if t.is_floating_point() else t)(torch.as_tensor(a)))
    B, Hq, Hkv, Sq, Sk, D = 8, 32, 4, 48, 2048, 128
    return {
        "q": _w((lambda *s: torch.randn(*s, dtype=torch.bfloat16) * 0.8)(B, Hq, Sq, D)),
        "k": _w((lambda *s: torch.randn(*s, dtype=torch.bfloat16) * 0.8)(B, Hkv, Sk, D)),
        "v": _w((lambda *s: torch.randn(*s, dtype=torch.bfloat16) * 0.05)(B, Hkv, Sk, D)),
    }

def reference(q, k, v):
    B, Hq, Sq, D = q.shape
    Hkv = k.shape[1]
    rep = Hq // Hkv
    k = (lambda x, n, axis: torch.repeat_interleave(x, n, dim=axis))(k, rep, axis=1)
    v = (lambda x, n, axis: torch.repeat_interleave(x, n, dim=axis))(v, rep, axis=1)
    scale = D ** -0.5
    scores = torch.einsum("bhqd,bhkd->bhqk", q, k) * scale
    p = _softmax(scores, axis=-1)
    return torch.einsum("bhqk,bhkd->bhqd", p, v)
```

```cpp
#include <hip/hip_runtime.h>
#include <hip/hip_bf16.h>
#include <stdint.h>

typedef __attribute__((ext_vector_type(8))) short bf16x8;
typedef __attribute__((ext_vector_type(4))) short s16x4;
typedef __attribute__((ext_vector_type(4))) float f32x4;
typedef __attribute__((ext_vector_type(2))) float f32x2;
typedef __attribute__((ext_vector_type(4))) unsigned u32x4;
typedef __attribute__((ext_vector_type(2))) unsigned u32x2;

constexpr int NB_ = 8, HQ = 32, HKV = 4, SQ = 48, SK = 2048, DH = 128;
constexpr int NW = 8;
constexpr int KPW = SK / NW;
constexpr int KB = 32;
constexpr int NBLK = KPW / KB;
constexpr int PFD = 3;
constexpr float CSC = 0.088388347648318440f * 1.4426950408889634f;
constexpr float THR2 = 10.0f;

constexpr int LDS_KV = NW * 16384;
constexpr int LDS_PF = LDS_KV;
constexpr int LDS_ST = LDS_PF + NW * 256;
constexpr int LDS_TOTAL = LDS_ST + NW * SQ * 2 * 4;

__device__ __forceinline__ unsigned cvtpk(float lo, float hi) {
  unsigned r; asm("v_cvt_pk_bf16_f32 %0, %1, %2" : "=v"(r) : "v"(lo), "v"(hi)); return r;
}
__device__ __forceinline__ float xmax_g(float x) {
  auto a = __builtin_amdgcn_permlane16_swap(__float_as_uint(x), __float_as_uint(x), false, false);
  x = fmaxf(__uint_as_float(a[0]), __uint_as_float(a[1]));
  auto b = __builtin_amdgcn_permlane32_swap(__float_as_uint(x), __float_as_uint(x), false, false);
  return fmaxf(__uint_as_float(b[0]), __uint_as_float(b[1]));
}
__device__ __forceinline__ float xsum_g(float x) {
  auto a = __builtin_amdgcn_permlane16_swap(__float_as_uint(x), __float_as_uint(x), false, false);
  x = __uint_as_float(a[0]) + __uint_as_float(a[1]);
  auto b = __builtin_amdgcn_permlane32_swap(__float_as_uint(x), __float_as_uint(x), false, false);
  return __uint_as_float(b[0]) + __uint_as_float(b[1]);
}

__device__ __forceinline__ void dma_block(unsigned lds0, unsigned so0, __amdgpu_buffer_rsrc_t srd,
                                          unsigned v0, unsigned v1, unsigned v2, unsigned v3) {
  asm volatile(
      "s_waitcnt lgkmcnt(0)\n\t"
      "s_mov_b32 m0, %0\n\t"
      "s_nop 0\n\t"
      "buffer_load_dwordx4 %4, %3, %1 offen offset:0 lds\n\t"
      "buffer_load_dwordx4 %5, %3, %1 offen offset:1024 lds\n\t"
      "buffer_load_dwordx4 %6, %3, %1 offen offset:2048 lds\n\t"
      "buffer_load_dwordx4 %7, %3, %1 offen offset:3072 lds\n\t"
      "s_add_u32 m0, %0, 4096\n\t"
      "s_nop 0\n\t"
      "buffer_load_dwordx4 %4, %3, %2 offen offset:0 lds\n\t"
      "buffer_load_dwordx4 %5, %3, %2 offen offset:1024 lds\n\t"
      "buffer_load_dwordx4 %6, %3, %2 offen offset:2048 lds\n\t"
      "buffer_load_dwordx4 %7, %3, %2 offen offset:3072 lds"
      :: "s"(lds0), "s"(so0), "s"(so0 + 4096u), "s"(srd), "v"(v0), "v"(v1), "v"(v2), "v"(v3)
      : "memory", "scc");
}
__device__ __forceinline__ void prefetch_l2(unsigned ldsdummy, const char* p) {
  asm volatile("s_mov_b32 m0, %0\n\ts_nop 0\n\tglobal_load_lds_dword %1, off" :: "s"(ldsdummy), "v"(p) : "memory");
}
template <int N> __device__ __forceinline__ void wait_vm() { asm volatile("s_waitcnt vmcnt(%0)" :: "i"(N) : "memory"); }

typedef __attribute__((address_space(3))) s16x4 lds_s16x4;

__global__ __launch_bounds__(NW * 64, 2)
void gqa_fwd_kernel(const __hip_bfloat16* __restrict__ q, const __hip_bfloat16* __restrict__ k,
                    const __hip_bfloat16* __restrict__ v, __hip_bfloat16* __restrict__ o) {
  __shared__ __attribute__((aligned(1024))) char smem[LDS_TOTAL];
  const int tid = threadIdx.x, lane = tid & 63;
  const int wid = __builtin_amdgcn_readfirstlane(tid >> 6);
  const int c = lane & 15, g = lane >> 4;
  const int bid = blockIdx.x, xcd = bid & 7, idx = bid >> 3;
  const int grp = xcd * 4 + (idx >> 3), hh = idx & 7;
  const int qhead = grp * 8 + hh;
  const char* Kg = (const char*)k + (size_t)grp * SK * DH * 2;
  const char* Vg = (const char*)v + (size_t)grp * SK * DH * 2;
  const __amdgpu_buffer_rsrc_t srdK = __builtin_amdgcn_make_buffer_rsrc((void*)Kg, (short)0, SK * DH * 2, 0x00020000);
  const __amdgpu_buffer_rsrc_t srdV = __builtin_amdgcn_make_buffer_rsrc((void*)Vg, (short)0, SK * DH * 2, 0x00020000);

  const unsigned lds_base = (unsigned)(uintptr_t)smem;
  const unsigned ldsK = lds_base + wid * 16384, ldsV = ldsK + 8192, ldsP = lds_base + LDS_PF + wid * 256;
  const unsigned rr = lane >> 4, pos = lane & 15;
  const unsigned kv0 = rr * 256 + ((pos ^ (0 + rr)) << 4), kv1 = rr * 256 + ((pos ^ (4 + rr)) << 4);
  const unsigned kv2 = rr * 256 + ((pos ^ (8 + rr)) << 4), kv3 = rr * 256 + ((pos ^ (12 + rr)) << 4);
  const unsigned vv0 = rr * 256 + ((pos ^ (2 * rr)) << 4), vv1 = rr * 256 + ((pos ^ (2 * (4 + rr))) << 4);
  const unsigned so_w = (unsigned)wid * KPW * 256;

  dma_block(ldsK, so_w, srdK, kv0, kv1, kv2, kv3);
  bf16x8 qf[3][4];
  {
    const __hip_bfloat16* qp = q + ((size_t)qhead * SQ + c) * DH + g * 8;
#pragma unroll
    for (int t = 0; t < 3; ++t)
#pragma unroll
      for (int s = 0; s < 4; ++s) qf[t][s] = *reinterpret_cast<const bf16x8*>(qp + t * 16 * DH + s * 32);
  }
  dma_block(ldsV, so_w, srdV, vv0, vv1, vv0, vv1);
  const char* pfp = (lane < 32 ? Kg : Vg) + so_w + hh * 1024 + (lane & 31) * 32;
#pragma unroll
  for (int j = 1; j < PFD; ++j) prefetch_l2(ldsP, pfp + j * 8192);

  const char* kr = smem + wid * 16384 + c * 256;
  unsigned ka[4];
#pragma unroll
  for (int s = 0; s < 4; ++s) ka[s] = (unsigned)(((4 * s + g) ^ c) << 4);
  const int qq = (lane & 15) >> 2, pp = lane & 3;
  const int vkey = 4 * g + qq, k7 = vkey & 7;
  const char* vr = smem + wid * 16384 + 8192 + vkey * 256 + (pp >> 1) * 16 + (pp & 1) * 8;

  f32x4 oacc[8][3];
#pragma unroll
  for (int n = 0; n < 8; ++n)
#pragma unroll
    for (int t = 0; t < 3; ++t) oacc[n][t] = f32x4{0.f, 0.f, 0.f, 0.f};
  float mC[3] = {0.f, 0.f, 0.f}, lsum[3] = {0.f, 0.f, 0.f};

#pragma unroll 1
  for (int j = 0; j < NBLK; ++j) {
    if (j == 0) wait_vm<8 + PFD - 1>(); else if (j + PFD - 1 < NBLK) wait_vm<9>(); else wait_vm<8>();
    f32x4 sc[2][3];
#pragma unroll
    for (int u = 0; u < 2; ++u) {
#pragma unroll
      for (int t = 0; t < 3; ++t) sc[u][t] = f32x4{0.f, 0.f, 0.f, 0.f};
#pragma unroll
      for (int s = 0; s < 4; ++s) {
        const bf16x8 kf = *reinterpret_cast<const bf16x8*>(kr + u * 4096 + ka[s]);
#pragma unroll
        for (int t = 0; t < 3; ++t) sc[u][t] = __builtin_amdgcn_mfma_f32_16x16x32_bf16(kf, qf[t][s], sc[u][t], 0, 0, 0);
      }
    }
    if (j + 1 < NBLK) dma_block(ldsK, so_w + (j + 1) * 8192, srdK, kv0, kv1, kv2, kv3);
    float x[3][8];
    float xm = -3.0e38f;
#pragma unroll
    for (int t = 0; t < 3; ++t)
#pragma unroll
      for (int u = 0; u < 2; ++u)
#pragma unroll
        for (int r = 0; r < 4; ++r) { x[t][u * 4 + r] = fmaf(sc[u][t][r], CSC, -mC[t]); xm = fmaxf(xm, x[t][u * 4 + r]); }
    if (j == 0 || __any(!(xm <= THR2))) {
#pragma unroll
      for (int t = 0; t < 3; ++t) {
        float m = x[t][0];
#pragma unroll
        for (int e = 1; e < 8; ++e) m = fmaxf(m, x[t][e]);
        m = xmax_g(m);
        if (j != 0) {
          m = fmaxf(m, 0.f);
          const float al = __builtin_amdgcn_exp2f(-m);
          lsum[t] *= al;
#pragma unroll
          for (int n = 0; n < 8; ++n) oacc[n][t] *= al;
        }
        mC[t] += m;
#pragma unroll
        for (int e = 0; e < 8; ++e) x[t][e] -= m;
      }
    }
    bf16x8 pf[3];
#pragma unroll
    for (int t = 0; t < 3; ++t) {
      float p[8];
#pragma unroll
      for (int e = 0; e < 8; ++e) { p[e] = __builtin_amdgcn_exp2f(x[t][e]); lsum[t] += p[e]; }
      u32x4 w = {cvtpk(p[0], p[1]), cvtpk(p[2], p[3]), cvtpk(p[4], p[5]), cvtpk(p[6], p[7])};
      pf[t] = *reinterpret_cast<bf16x8*>(&w);
    }
    if (j + 1 < NBLK) { if (j == 0) wait_vm<8 + PFD - 1>(); else if (j + PFD - 1 < NBLK) wait_vm<9>(); else wait_vm<8>(); }
    else wait_vm<0>();
#pragma unroll
    for (int n = 0; n < 8; ++n) {
      const char* a = vr + ((n ^ k7) << 5);
      const s16x4 lo = __builtin_amdgcn_ds_read_tr16_b64_v4i16((lds_s16x4*)(a));
      const s16x4 hi = __builtin_amdgcn_ds_read_tr16_b64_v4i16((lds_s16x4*)(a + 4096));
      const bf16x8 vf = {lo[0], lo[1], lo[2], lo[3], hi[0], hi[1], hi[2], hi[3]};
#pragma unroll
      for (int t = 0; t < 3; ++t) oacc[n][t] = __builtin_amdgcn_mfma_f32_16x16x32_bf16(vf, pf[t], oacc[n][t], 0, 0, 0);
    }
    if (j + 1 < NBLK) {
      dma_block(ldsV, so_w + (j + 1) * 8192, srdV, vv0, vv1, vv0, vv1);
      if (j + PFD < NBLK) prefetch_l2(ldsP, pfp + (j + PFD) * 8192);
    }
  }

  float* st = reinterpret_cast<float*>(smem + LDS_ST);
#pragma unroll
  for (int t = 0; t < 3; ++t) {
    lsum[t] = xsum_g(lsum[t]);
    if (g == 0) *reinterpret_cast<f32x2*>(st + (wid * SQ + t * 16 + c) * 2) = f32x2{mC[t], lsum[t]};
  }
  __syncthreads();
#pragma unroll
  for (int t = 0; t < 3; ++t) {
    f32x2 s8[NW];
#pragma unroll
    for (int w = 0; w < NW; ++w) s8[w] = *reinterpret_cast<const f32x2*>(st + (w * SQ + t * 16 + c) * 2);
    float M = s8[0][0];
#pragma unroll
    for (int w = 1; w < NW; ++w) M = fmaxf(M, s8[w][0]);
    float L = 0.f;
#pragma unroll
    for (int w = 0; w < NW; ++w) L += s8[w][1] * __builtin_amdgcn_exp2f(s8[w][0] - M);
    const float f = __builtin_amdgcn_exp2f(mC[t] - M) / L;
#pragma unroll
    for (int n = 0; n < 8; ++n) oacc[n][t] *= f;
  }
  f32x4* red = reinterpret_cast<f32x4*>(smem);
  __hip_bfloat16* op = o + ((size_t)qhead * SQ + c) * DH + wid * 16 + g * 4;
#pragma unroll
  for (int t = 0; t < 2; ++t)
#pragma unroll
    for (int n = 0; n < 8; ++n) red[((t * 8 + n) * 8 + wid) * 64 + lane] = oacc[n][t];
  __syncthreads();
#pragma unroll
  for (int t = 0; t < 2; ++t) {
    f32x4 a = red[((t * 8 + wid) * 8 + 0) * 64 + lane];
#pragma unroll
    for (int w = 1; w < NW; ++w) a += red[((t * 8 + wid) * 8 + w) * 64 + lane];
    u32x2 pk = {cvtpk(a[0], a[1]), cvtpk(a[2], a[3])};
    *reinterpret_cast<u32x2*>(op + t * 16 * DH) = pk;
  }
  __syncthreads();
#pragma unroll
  for (int n = 0; n < 8; ++n) red[(n * 8 + wid) * 64 + lane] = oacc[n][2];
  __syncthreads();
  {
    f32x4 a = red[(wid * 8 + 0) * 64 + lane];
#pragma unroll
    for (int w = 1; w < NW; ++w) a += red[(wid * 8 + w) * 64 + lane];
    u32x2 pk = {cvtpk(a[0], a[1]), cvtpk(a[2], a[3])};
    *reinterpret_cast<u32x2*>(op + 2 * 16 * DH) = pk;
  }
}

extern "C" void kernel_launch(void* const* d_in, const int* in_sizes, int n_in,
                              void* d_out, int out_size, void* d_ws, size_t ws_size,
                              hipStream_t stream) {
  const __hip_bfloat16* q = (const __hip_bfloat16*)d_in[0];
  const __hip_bfloat16* k = (const __hip_bfloat16*)d_in[1];
  const __hip_bfloat16* v = (const __hip_bfloat16*)d_in[2];
  __hip_bfloat16* o = (__hip_bfloat16*)d_out;
  gqa_fwd_kernel<<<dim3(NB_ * HQ), dim3(NW * 64), 0, stream>>>(q, k, v, o);
}
```

```cpp
#include <hip/hip_runtime.h>
#include <hip/hip_bf16.h>
#include <stdint.h>
#include <type_traits>

typedef __attribute__((ext_vector_type(8))) short bf16x8;
typedef __attribute__((ext_vector_type(4))) short s16x4;
typedef __attribute__((ext_vector_type(4))) float f32x4;
typedef __attribute__((ext_vector_type(2))) float f32x2;
typedef __attribute__((ext_vector_type(4))) unsigned u32x4;
typedef __attribute__((ext_vector_type(2))) unsigned u32x2;

constexpr int NB_ = 8, HQ = 32, HKV = 4, SQ = 48, SK = 2048, DH = 128;
constexpr int NW = 8;
constexpr int KPW = SK / NW;
constexpr int KB = 32;
constexpr int NBLK = KPW / KB;
#define PFD_ 3
constexpr int PFD = PFD_;
constexpr float CSC = 0.088388347648318440f * 1.4426950408889634f;
constexpr float THR2 = 10.0f;

constexpr int LDS_KV = NW * 16384;
constexpr int LDS_Q = LDS_KV;
constexpr int LDS_PF = LDS_Q + SQ * 256;
constexpr int LDS_ST = LDS_PF + NW * 256;
constexpr int LDS_TOTAL = LDS_ST + NW * SQ * 2 * 4;

#define STAMP(x)

typedef __attribute__((ext_vector_type(2))) __bf16 bf16x2;
__device__ __forceinline__ unsigned cvtpk(float lo, float hi) {
  f32x2 v = {lo, hi};
  return __builtin_bit_cast(unsigned, __builtin_convertvector(v, bf16x2));
}
__device__ __forceinline__ float xmax_g(float x) {
  auto a = __builtin_amdgcn_permlane16_swap(__float_as_uint(x), __float_as_uint(x), false, false);
  x = fmaxf(__uint_as_float(a[0]), __uint_as_float(a[1]));
  auto b = __builtin_amdgcn_permlane32_swap(__float_as_uint(x), __float_as_uint(x), false, false);
  return fmaxf(__uint_as_float(b[0]), __uint_as_float(b[1]));
}

__device__ __forceinline__ float xsum_g(float x) {
  auto a = __builtin_amdgcn_permlane16_swap(__float_as_uint(x), __float_as_uint(x), false, false);
  x = __uint_as_float(a[0]) + __uint_as_float(a[1]);
  auto b = __builtin_amdgcn_permlane32_swap(__float_as_uint(x), __float_as_uint(x), false, false);
  return __uint_as_float(b[0]) + __uint_as_float(b[1]);
}

__device__ __forceinline__ void dma_block(unsigned lds0, unsigned so0, __amdgpu_buffer_rsrc_t srd,
                                          unsigned v0, unsigned v1, unsigned v2, unsigned v3) {
  asm volatile(
      "s_waitcnt lgkmcnt(0)\n\t"
      "s_mov_b32 m0, %0\n\t"
      "s_nop 0\n\t"
      "buffer_load_dwordx4 %4, %3, %1 offen offset:0 nt lds\n\t"
      "buffer_load_dwordx4 %5, %3, %1 offen offset:1024 nt lds\n\t"
      "buffer_load_dwordx4 %6, %3, %1 offen offset:2048 nt lds\n\t"
      "buffer_load_dwordx4 %7, %3, %1 offen offset:3072 nt lds\n\t"
      "s_add_u32 m0, %0, 4096\n\t"
      "s_nop 0\n\t"
      "buffer_load_dwordx4 %4, %3, %2 offen offset:0 nt lds\n\t"
      "buffer_load_dwordx4 %5, %3, %2 offen offset:1024 nt lds\n\t"
      "buffer_load_dwordx4 %6, %3, %2 offen offset:2048 nt lds\n\t"
      "buffer_load_dwordx4 %7, %3, %2 offen offset:3072 nt lds"
      :: "s"(lds0), "s"(so0), "s"(so0 + 4096u), "s"(srd), "v"(v0), "v"(v1), "v"(v2), "v"(v3)
      : "memory", "scc");
}
__device__ __forceinline__ void dma_piece(unsigned lds0, unsigned so0, __amdgpu_buffer_rsrc_t srd, unsigned v0) {
  asm volatile("s_mov_b32 m0, %0\n\ts_nop 0\n\tbuffer_load_dwordx4 %3, %2, %1 offen nt lds"
               :: "s"(lds0), "s"(so0), "s"(srd), "v"(v0) : "memory");
}
__device__ __forceinline__ void prefetch_l2(unsigned ldsdummy, const char* p) {
  asm volatile("s_mov_b32 m0, %0\n\ts_nop 0\n\tglobal_load_lds_dword %1, off nt" :: "s"(ldsdummy), "v"(p) : "memory");
}
template <int N> __device__ __forceinline__ void wait_vm() { asm volatile("s_waitcnt vmcnt(%0)" :: "i"(N) : "memory"); }

typedef __attribute__((address_space(3))) s16x4 lds_s16x4;
typedef __attribute__((address_space(3))) bf16x8 lds_bf16x8;

__global__ __launch_bounds__(NW * 64, 2)
void gqa_fwd_kernel(const __hip_bfloat16* __restrict__ q, const __hip_bfloat16* __restrict__ k,
                    const __hip_bfloat16* __restrict__ v, __hip_bfloat16* __restrict__ o
                    ) {
  __shared__ __attribute__((aligned(1024))) char smem[LDS_TOTAL];
  STAMP(T0);
  const int tid = threadIdx.x, lane = tid & 63;
  const int wid = __builtin_amdgcn_readfirstlane(tid >> 6);
  const int c = lane & 15, g = lane >> 4;
  const int bid = blockIdx.x, xcd = bid & 7, idx = bid >> 3;
  const int grp = xcd * 4 + (idx >> 3), hh = idx & 7;
  const int qhead = grp * 8 + hh;
  const char* Kg = (const char*)k + (size_t)grp * SK * DH * 2;
  const char* Vg = (const char*)v + (size_t)grp * SK * DH * 2;
  const char* Qg = (const char*)q + (size_t)qhead * SQ * DH * 2;
  const __amdgpu_buffer_rsrc_t srdK = __builtin_amdgcn_make_buffer_rsrc((void*)Kg, (short)0, SK * DH * 2, 0x00020000);
  const __amdgpu_buffer_rsrc_t srdV = __builtin_amdgcn_make_buffer_rsrc((void*)Vg, (short)0, SK * DH * 2, 0x00020000);
  const __amdgpu_buffer_rsrc_t srdQ = __builtin_amdgcn_make_buffer_rsrc((void*)Qg, (short)0, SQ * DH * 2, 0x00020000);

  const unsigned lds_base = (unsigned)(uintptr_t)smem;
  const unsigned ldsK = lds_base + wid * 16384, ldsV = ldsK + 8192, ldsP = lds_base + LDS_PF + wid * 256;
  const unsigned rr = lane >> 4, pos = lane & 15;
  const unsigned kv0 = rr * 256 + ((pos ^ (0 + rr)) << 4), kv1 = rr * 256 + ((pos ^ (4 + rr)) << 4);
  const unsigned kv2 = rr * 256 + ((pos ^ (8 + rr)) << 4), kv3 = rr * 256 + ((pos ^ (12 + rr)) << 4);
  const unsigned vv0 = rr * 256 + ((pos ^ (2 * rr)) << 4), vv1 = rr * 256 + ((pos ^ (2 * (4 + rr))) << 4);
  const unsigned so_w = (unsigned)wid * KPW * 256;

  {
    const unsigned qv = rr * 256 + ((pos ^ (4 * (wid & 3) + rr)) << 4);
    dma_piece(lds_base + LDS_Q + wid * 1024, wid * 1024, srdQ, qv);
    if (wid < 4) dma_piece(lds_base + LDS_Q + (wid + 8) * 1024, (wid + 8) * 1024, srdQ, qv);
  }
  dma_block(ldsK, so_w, srdK, kv0, kv1, kv2, kv3);
  dma_block(ldsV, so_w, srdV, vv0, vv1, vv0, vv1);
  wait_vm<16>();

  unsigned ka[4];
#pragma unroll
  for (int s = 0; s < 4; ++s) ka[s] = (unsigned)(c * 256 + (((4 * s + g) ^ c) << 4));
  const char* kr = smem + wid * 16384;
#define PRIO 0
  if (PRIO == 1 && wid >= 4) __builtin_amdgcn_s_setprio(1);
  const int qq = (lane & 15) >> 2, pp = lane & 3;
  const int vkey = 4 * g + qq, k7 = vkey & 7;
  const char* vr = smem + wid * 16384 + 8192 + vkey * 256 + (pp >> 1) * 16 + (pp & 1) * 8;

  __builtin_amdgcn_s_barrier();
  asm volatile("" ::: "memory");
  bf16x8 qf[3][4];
#pragma unroll
  for (int t = 0; t < 3; ++t)
#pragma unroll
    for (int s = 0; s < 4; ++s) qf[t][s] = *reinterpret_cast<const bf16x8*>(smem + LDS_Q + t * 4096 + ka[s]);

  f32x4 oacc[8][3], lacc[3];
#pragma unroll
  for (int t = 0; t < 3; ++t) {
    lacc[t] = f32x4{0.f, 0.f, 0.f, 0.f};
#pragma unroll
    for (int n = 0; n < 8; ++n) oacc[n][t] = f32x4{0.f, 0.f, 0.f, 0.f};
  }
  float mC[3] = {0.f, 0.f, 0.f};
  const bf16x8 ones = {0x3F80, 0x3F80, 0x3F80, 0x3F80, 0x3F80, 0x3F80, 0x3F80, 0x3F80};
  STAMP(T1);
  wait_vm<8>();
  bf16x8 kf[2][4];
#pragma unroll
  for (int u = 0; u < 2; ++u)
#pragma unroll
    for (int s = 0; s < 4; ++s) kf[u][s] = *reinterpret_cast<const bf16x8*>(kr + u * 4096 + ka[s]);

  unsigned kvb = kv0, vvb = vv0;
  unsigned kab = ldsK + ka[0];
  unsigned vab = ldsV + vkey * 256 + (k7 << 5) + (pp >> 1) * 16 + (pp & 1) * 8;
#define SB() __builtin_amdgcn_sched_barrier(0)
#define MFMA(a, b, c) __builtin_amdgcn_mfma_f32_16x16x32_bf16(a, b, c, 0, 0, 0)
#define QKM(S, t, i) S[(i) & 1] = MFMA(kf[(i) & 1][(i) >> 1], qf[t][(i) >> 1], S[(i) & 1])
#define XF(X, S, t, e) X[e] = fmaf(S[(e) >> 2][(e) & 3], CSC, -mC[t])
#define EX(P, X, e) P[e] = __builtin_amdgcn_exp2f(X[e])
#define PVM(t, n, pf) oacc[n][t] = MFMA(vf[n], pf, oacc[n][t])
  auto body = [&](auto FIRST_, auto LAST_, int j) {
    constexpr bool FIRST = decltype(FIRST_)::value, LAST = decltype(LAST_)::value;
    f32x4 s0[2] = {f32x4{0.f, 0.f, 0.f, 0.f}, f32x4{0.f, 0.f, 0.f, 0.f}};
    f32x4 s1[2] = {f32x4{0.f, 0.f, 0.f, 0.f}, f32x4{0.f, 0.f, 0.f, 0.f}};
    f32x4 s2[2] = {f32x4{0.f, 0.f, 0.f, 0.f}, f32x4{0.f, 0.f, 0.f, 0.f}};
    float x0[8], x1[8], x2[8], e0[8], e1[8], e2[8];
    u32x4 w0, w1, w2;
    float mx;
    asm volatile("" : "+v"(kvb), "+v"(vvb), "+v"(kab), "+v"(vab));
    QKM(s0, 0, 0); QKM(s0, 0, 1); SB();
    if constexpr (!LAST) dma_block(ldsK, so_w + (j + 1) * 8192, srdK, kvb, kvb ^ 64u, kvb ^ 128u, kvb ^ 192u);
    SB();
    QKM(s0, 0, 2); QKM(s0, 0, 3); QKM(s0, 0, 4); QKM(s0, 0, 5); QKM(s0, 0, 6); QKM(s0, 0, 7); SB();
    QKM(s1, 1, 0); SB();
    QKM(s1, 1, 1); SB();
    QKM(s1, 1, 2); XF(x0, s0, 0, 0); XF(x0, s0, 0, 1); SB();
    QKM(s1, 1, 3); XF(x0, s0, 0, 2); XF(x0, s0, 0, 3); SB();
    QKM(s1, 1, 4); XF(x0, s0, 0, 4); XF(x0, s0, 0, 5); SB();
    QKM(s1, 1, 5); XF(x0, s0, 0, 6); XF(x0, s0, 0, 7); SB();
    QKM(s1, 1, 6); EX(e0, x0, 0); SB();
    QKM(s1, 1, 7); EX(e0, x0, 1); SB();
    QKM(s2, 2, 0); EX(e0, x0, 2); SB();
    QKM(s2, 2, 1); EX(e0, x0, 3); SB();
    QKM(s2, 2, 2); EX(e0, x0, 4); SB();
    QKM(s2, 2, 3); EX(e0, x0, 5); SB();
    QKM(s2, 2, 4); EX(e0, x0, 6); SB();
    QKM(s2, 2, 5); EX(e0, x0, 7); SB();
    QKM(s2, 2, 6); XF(x1, s1, 1, 0); XF(x1, s1, 1, 1); SB();
    QKM(s2, 2, 7); XF(x1, s1, 1, 2); XF(x1, s1, 1, 3); SB();
    if constexpr (!LAST) wait_vm<8>(); else wait_vm<0>();
    bf16x8 vf[8];
#pragma unroll
    for (int n = 0; n < 8; ++n) {
      const unsigned a = vab ^ (unsigned)(n << 5);
      const s16x4 lo = __builtin_amdgcn_ds_read_tr16_b64_v4i16((lds_s16x4*)(uintptr_t)(a));
      const s16x4 hi = __builtin_amdgcn_ds_read_tr16_b64_v4i16((lds_s16x4*)(uintptr_t)(a + 4096));
      vf[n] = bf16x8{lo[0], lo[1], lo[2], lo[3], hi[0], hi[1], hi[2], hi[3]};
    }
    SB();
    XF(x1, s1, 1, 4); XF(x1, s1, 1, 5); XF(x1, s1, 1, 6); XF(x1, s1, 1, 7);
    w0 = u32x4{cvtpk(e0[0], e0[1]), cvtpk(e0[2], e0[3]), cvtpk(e0[4], e0[5]), cvtpk(e0[6], e0[7])};
#pragma unroll
    for (int e = 0; e < 8; ++e) XF(x2, s2, 2, e);
    mx = x0[0];
#pragma unroll
    for (int e = 1; e < 8; ++e) mx = fmaxf(mx, x0[e]);
#pragma unroll
    for (int e = 0; e < 8; ++e) mx = fmaxf(mx, fmaxf(x1[e], x2[e]));
    if (FIRST || __any(!(mx <= THR2))) {
      auto fix = [&](int t, float (&x)[8]) {
        float m = x[0];
#pragma unroll
        for (int e = 1; e < 8; ++e) m = fmaxf(m, x[e]);
        m = xmax_g(m);
        if constexpr (!FIRST) {
          m = fmaxf(m, 0.f);
          const float al = __builtin_amdgcn_exp2f(-m);
          lacc[t] *= al;
#pragma unroll
          for (int n = 0; n < 8; ++n) oacc[n][t] *= al;
        }
        mC[t] += m;
#pragma unroll
        for (int e = 0; e < 8; ++e) x[e] -= m;
      };
      fix(0, x0); fix(1, x1); fix(2, x2);
#pragma unroll
      for (int e = 0; e < 8; ++e) EX(e0, x0, e);
      w0 = u32x4{cvtpk(e0[0], e0[1]), cvtpk(e0[2], e0[3]), cvtpk(e0[4], e0[5]), cvtpk(e0[6], e0[7])};
    }
    const bf16x8 p0 = *reinterpret_cast<bf16x8*>(&w0);
    SB();
    lacc[0] = MFMA(ones, p0, lacc[0]); EX(e1, x1, 0); SB();
    PVM(0, 0, p0); EX(e1, x1, 1); SB();
    PVM(0, 1, p0); EX(e1, x1, 2); SB();
    PVM(0, 2, p0); EX(e1, x1, 3); SB();
    PVM(0, 3, p0); EX(e1, x1, 4); SB();
    PVM(0, 4, p0); EX(e1, x1, 5); SB();
    PVM(0, 5, p0); EX(e1, x1, 6); SB();
    PVM(0, 6, p0); EX(e1, x1, 7); SB();
    PVM(0, 7, p0); w1[0] = cvtpk(e1[0], e1[1]); w1[1] = cvtpk(e1[2], e1[3]); SB();
    if constexpr (!LAST) {
      dma_block(ldsV, so_w + (j + 1) * 8192, srdV, vvb, vvb ^ 128u, vvb, vvb ^ 128u);
    }
    w1[2] = cvtpk(e1[4], e1[5]); w1[3] = cvtpk(e1[6], e1[7]); EX(e2, x2, 0);
    const bf16x8 p1 = *reinterpret_cast<bf16x8*>(&w1);
    SB();
    lacc[1] = MFMA(ones, p1, lacc[1]); EX(e2, x2, 1); SB();
    PVM(1, 0, p1); EX(e2, x2, 2); SB();
    PVM(1, 1, p1); EX(e2, x2, 3); SB();
    PVM(1, 2, p1); EX(e2, x2, 4); SB();
    PVM(1, 3, p1); EX(e2, x2, 5); SB();
    PVM(1, 4, p1); EX(e2, x2, 6); SB();
    PVM(1, 5, p1); EX(e2, x2, 7); SB();
    PVM(1, 6, p1); w2[0] = cvtpk(e2[0], e2[1]); w2[1] = cvtpk(e2[2], e2[3]); SB();
    PVM(1, 7, p1); w2[2] = cvtpk(e2[4], e2[5]); w2[3] = cvtpk(e2[6], e2[7]); SB();
    const bf16x8 p2 = *reinterpret_cast<bf16x8*>(&w2);
    if constexpr (!LAST) {
      wait_vm<8>();
#pragma unroll
      for (int u = 0; u < 2; ++u)
#pragma unroll
        for (int s = 0; s < 4; ++s) kf[u][s] = *(const lds_bf16x8*)(uintptr_t)((kab ^ (unsigned)(s << 6)) + u * 4096);
    }
    SB();
    lacc[2] = MFMA(ones, p2, lacc[2]);
#pragma unroll
    for (int n = 0; n < 8; ++n) PVM(2, n, p2);
    SB();
  };
  body(std::true_type{}, std::false_type{}, 0);
#pragma unroll 1
  for (int j = 1; j < NBLK - 1; ++j) body(std::false_type{}, std::false_type{}, j);
  body(std::false_type{}, std::true_type{}, NBLK - 1);
  STAMP(T2);

  float* st = reinterpret_cast<float*>(smem + LDS_ST);
#pragma unroll
  for (int t = 0; t < 3; ++t)
    if (g == 0) *reinterpret_cast<f32x2*>(st + (wid * SQ + t * 16 + c) * 2) = f32x2{mC[t], lacc[t][0]};
  {
    u32x4* mine = reinterpret_cast<u32x4*>(smem + wid * 16384);
#pragma unroll
    for (int n = 0; n < 8; ++n)
      mine[n * 64 + lane] = u32x4{cvtpk(oacc[n][0][0], oacc[n][0][1]), cvtpk(oacc[n][0][2], oacc[n][0][3]),
                                  cvtpk(oacc[n][1][0], oacc[n][1][1]), cvtpk(oacc[n][1][2], oacc[n][1][3])};
    u32x2* mine2 = reinterpret_cast<u32x2*>(smem + wid * 16384 + 8192);
#pragma unroll
    for (int n = 0; n < 8; ++n)
      mine2[n * 64 + lane] = u32x2{cvtpk(oacc[n][2][0], oacc[n][2][1]), cvtpk(oacc[n][2][2], oacc[n][2][3])};
  }
  STAMP(T3);
  __syncthreads();
  STAMP(T4);
  u32x4 pa[NW]; u32x2 pb[NW];
#pragma unroll
  for (int w = 0; w < NW; ++w) {
    pa[w] = reinterpret_cast<const u32x4*>(smem + w * 16384)[wid * 64 + lane];
    pb[w] = reinterpret_cast<const u32x2*>(smem + w * 16384 + 8192)[wid * 64 + lane];
  }
  __hip_bfloat16* op = o + ((size_t)qhead * SQ + c) * DH + wid * 16 + g * 4;
#pragma unroll
  for (int t = 0; t < 3; ++t) {
    f32x2 s8[NW];
#pragma unroll
    for (int w = 0; w < NW; ++w) s8[w] = *reinterpret_cast<const f32x2*>(st + (w * SQ + t * 16 + c) * 2);
    float M = s8[0][0];
#pragma unroll
    for (int w = 1; w < NW; ++w) M = fmaxf(M, s8[w][0]);
    float L = 0.f;
    f32x4 a = {0.f, 0.f, 0.f, 0.f};
#pragma unroll
    for (int w = 0; w < NW; ++w) {
      const float f = __builtin_amdgcn_exp2f(s8[w][0] - M);
      L = fmaf(s8[w][1], f, L);
      const unsigned lo = t == 0 ? pa[w][0] : t == 1 ? pa[w][2] : pb[w][0];
      const unsigned hi = t == 0 ? pa[w][1] : t == 1 ? pa[w][3] : pb[w][1];
      a[0] = fmaf(__uint_as_float(lo << 16), f, a[0]);
      a[1] = fmaf(__uint_as_float(lo & 0xffff0000u), f, a[1]);
      a[2] = fmaf(__uint_as_float(hi << 16), f, a[2]);
      a[3] = fmaf(__uint_as_float(hi & 0xffff0000u), f, a[3]);
    }
    const float inv = 1.0f / L;
    u32x2 pk = {cvtpk(a[0] * inv, a[1] * inv), cvtpk(a[2] * inv, a[3] * inv)};
    *reinterpret_cast<u32x2*>(op + t * 16 * DH) = pk;
  }
  STAMP(T5);
  STAMP(T6);
}

extern "C" void kernel_launch(void* const* d_in, const int* in_sizes, int n_in,
                              void* d_out, int out_size, void* d_ws, size_t ws_size,
                              hipStream_t stream) {
  const __hip_bfloat16* q = (const __hip_bfloat16*)d_in[0];
  const __hip_bfloat16* k = (const __hip_bfloat16*)d_in[1];
  const __hip_bfloat16* v = (const __hip_bfloat16*)d_in[2];
  __hip_bfloat16* o = (__hip_bfloat16*)d_out;
  gqa_fwd_kernel<<<dim3(NB_ * HQ), dim3(NW * 64), 0, stream>>>(q, k, v, o
  );
}
```

```cpp
#include <hip/hip_runtime.h>
#include <hip/hip_bf16.h>
#include <stdint.h>
#include <type_traits>

typedef __attribute__((ext_vector_type(8))) short bf16x8;
typedef __attribute__((ext_vector_type(4))) short s16x4;
typedef __attribute__((ext_vector_type(4))) float f32x4;
typedef __attribute__((ext_vector_type(2))) float f32x2;
typedef __attribute__((ext_vector_type(4))) unsigned u32x4;
typedef __attribute__((ext_vector_type(2))) unsigned u32x2;

constexpr int NB_ = 8, HQ = 32, HKV = 4, SQ = 48, SK = 2048, DH = 128;
constexpr int NW = 8;
constexpr int KPW = SK / NW;
constexpr int KB = 32;
constexpr int NBLK = KPW / KB;
#define PFD_ 3
constexpr int PFD = PFD_;
constexpr float CSC = 0.088388347648318440f * 1.4426950408889634f;
constexpr float THR2 = 10.0f;

constexpr int LDS_KV = NW * 16384;
constexpr int LDS_Q = LDS_KV;
constexpr int LDS_PF = LDS_Q + SQ * 256;
constexpr int LDS_ST = LDS_PF + NW * 256;
constexpr int LDS_TOTAL = LDS_ST + NW * SQ * 2 * 4;

#define STAMP(x)

typedef __attribute__((ext_vector_type(2))) __bf16 bf16x2;
__device__ __forceinline__ unsigned cvtpk(float lo, float hi) {
  f32x2 v = {lo, hi};
  return __builtin_bit_cast(unsigned, __builtin_convertvector(v, bf16x2));
}
__device__ __forceinline__ float xmax_g(float x) {
  auto a = __builtin_amdgcn_permlane16_swap(__float_as_uint(x), __float_as_uint(x), false, false);
  x = fmaxf(__uint_as_float(a[0]), __uint_as_float(a[1]));
  auto b = __builtin_amdgcn_permlane32_swap(__float_as_uint(x), __float_as_uint(x), false, false);
  return fmaxf(__uint_as_float(b[0]), __uint_as_float(b[1]));
}

__device__ __forceinline__ float xsum_g(float x) {
  auto a = __builtin_amdgcn_permlane16_swap(__float_as_uint(x), __float_as_uint(x), false, false);
  x = __uint_as_float(a[0]) + __uint_as_float(a[1]);
  auto b = __builtin_amdgcn_permlane32_swap(__float_as_uint(x), __float_as_uint(x), false, false);
  return __uint_as_float(b[0]) + __uint_as_float(b[1]);
}

__device__ __forceinline__ void dma_block(unsigned lds0, unsigned so0, __amdgpu_buffer_rsrc_t srd,
                                          unsigned v0, unsigned v1, unsigned v2, unsigned v3) {
  asm volatile(
      "s_waitcnt lgkmcnt(0)\n\t"
      "s_mov_b32 m0, %0\n\t"
      "s_nop 0\n\t"
      "buffer_load_dwordx4 %4, %3, %1 offen offset:0 lds\n\t"
      "buffer_load_dwordx4 %5, %3, %1 offen offset:1024 lds\n\t"
      "buffer_load_dwordx4 %6, %3, %1 offen offset:2048 lds\n\t"
      "buffer_load_dwordx4 %7, %3, %1 offen offset:3072 lds\n\t"
      "s_add_u32 m0, %0, 4096\n\t"
      "s_nop 0\n\t"
      "buffer_load_dwordx4 %4, %3, %2 offen offset:0 lds\n\t"
      "buffer_load_dwordx4 %5, %3, %2 offen offset:1024 lds\n\t"
      "buffer_load_dwordx4 %6, %3, %2 offen offset:2048 lds\n\t"
      "buffer_load_dwordx4 %7, %3, %2 offen offset:3072 lds"
      :: "s"(lds0), "s"(so0), "s"(so0 + 4096u), "s"(srd), "v"(v0), "v"(v1), "v"(v2), "v"(v3)
      : "memory", "scc");
}
__device__ __forceinline__ void dma_piece(unsigned lds0, unsigned so0, __amdgpu_buffer_rsrc_t srd, unsigned v0) {
  asm volatile("s_mov_b32 m0, %0\n\ts_nop 0\n\tbuffer_load_dwordx4 %3, %2, %1 offen lds"
               :: "s"(lds0), "s"(so0), "s"(srd), "v"(v0) : "memory");
}
__device__ __forceinline__ void prefetch_l2(unsigned ldsdummy, const char* p) {
  asm volatile("s_mov_b32 m0, %0\n\ts_nop 0\n\tglobal_load_lds_dword %1, off nt" :: "s"(ldsdummy), "v"(p) : "memory");
}
template <int N> __device__ __forceinline__ void wait_vm() { asm volatile("s_waitcnt vmcnt(%0)" :: "i"(N) : "memory"); }

typedef __attribute__((address_space(3))) s16x4 lds_s16x4;
typedef __attribute__((address_space(3))) bf16x8 lds_bf16x8;

__global__ __launch_bounds__(NW * 64, 2)
void gqa_fwd_kernel(const __hip_bfloat16* __restrict__ q, const __hip_bfloat16* __restrict__ k,
                    const __hip_bfloat16* __restrict__ v, __hip_bfloat16* __restrict__ o
                    ) {
  __shared__ __attribute__((aligned(1024))) char smem[LDS_TOTAL];
  STAMP(T0);
  const int tid = threadIdx.x, lane = tid & 63;
  const int wid = __builtin_amdgcn_readfirstlane(tid >> 6);
  const int c = lane & 15, g = lane >> 4;
  const int bid = blockIdx.x, xcd = bid & 7, idx = bid >> 3;
  const int grp = xcd * 4 + (idx >> 3), hh = idx & 7;
  const int qhead = grp * 8 + hh;
  const char* Kg = (const char*)k + (size_t)grp * SK * DH * 2;
  const char* Vg = (const char*)v + (size_t)grp * SK * DH * 2;
  const char* Qg = (const char*)q + (size_t)qhead * SQ * DH * 2;
  const __amdgpu_buffer_rsrc_t srdK = __builtin_amdgcn_make_buffer_rsrc((void*)Kg, (short)0, SK * DH * 2, 0x00020000);
  const __amdgpu_buffer_rsrc_t srdV = __builtin_amdgcn_make_buffer_rsrc((void*)Vg, (short)0, SK * DH * 2, 0x00020000);
  const __amdgpu_buffer_rsrc_t srdQ = __builtin_amdgcn_make_buffer_rsrc((void*)Qg, (short)0, SQ * DH * 2, 0x00020000);

  const unsigned lds_base = (unsigned)(uintptr_t)smem;
  const unsigned ldsK = lds_base + wid * 16384, ldsV = ldsK + 8192, ldsP = lds_base + LDS_PF + wid * 256;
  const unsigned rr = lane >> 4, pos = lane & 15;
  const unsigned kv0 = rr * 256 + ((pos ^ (0 + rr)) << 4), kv1 = rr * 256 + ((pos ^ (4 + rr)) << 4);
  const unsigned kv2 = rr * 256 + ((pos ^ (8 + rr)) << 4), kv3 = rr * 256 + ((pos ^ (12 + rr)) << 4);
  const unsigned vv0 = rr * 256 + ((pos ^ (2 * rr)) << 4), vv1 = rr * 256 + ((pos ^ (2 * (4 + rr))) << 4);
  const unsigned so_w = (unsigned)wid * KPW * 256;

#define PROL 2
  {
    const unsigned qv = rr * 256 + ((pos ^ (4 * (wid & 3) + rr)) << 4);
    dma_piece(lds_base + LDS_Q + wid * 1024, wid * 1024, srdQ, qv);
    if (wid < 4) dma_piece(lds_base + LDS_Q + (wid + 8) * 1024, (wid + 8) * 1024, srdQ, qv);
  }
  __builtin_amdgcn_s_barrier();
  dma_block(ldsK, so_w, srdK, kv0, kv1, kv2, kv3);
  __builtin_amdgcn_s_barrier();
  dma_block(ldsV, so_w, srdV, vv0, vv1, vv0, vv1);
  wait_vm<16>();

  unsigned ka[4];
#pragma unroll
  for (int s = 0; s < 4; ++s) ka[s] = (unsigned)(c * 256 + (((4 * s + g) ^ c) << 4));
  const char* kr = smem + wid * 16384;
#define PRIO 0
  if (PRIO == 1 && wid >= 4) __builtin_amdgcn_s_setprio(1);
  const int qq = (lane & 15) >> 2, pp = lane & 3;
  const int vkey = 4 * g + qq, k7 = vkey & 7;
  const char* vr = smem + wid * 16384 + 8192 + vkey * 256 + (pp >> 1) * 16 + (pp & 1) * 8;

  __builtin_amdgcn_s_barrier();
  asm volatile("" ::: "memory");
  bf16x8 qf[3][4];
#pragma unroll
  for (int t = 0; t < 3; ++t)
#pragma unroll
    for (int s = 0; s < 4; ++s) qf[t][s] = *reinterpret_cast<const bf16x8*>(smem + LDS_Q + t * 4096 + ka[s]);

  f32x4 oacc[8][3], lacc[3];
#pragma unroll
  for (int t = 0; t < 3; ++t) {
    lacc[t] = f32x4{0.f, 0.f, 0.f, 0.f};
#pragma unroll
    for (int n = 0; n < 8; ++n) oacc[n][t] = f32x4{0.f, 0.f, 0.f, 0.f};
  }
  float mC[3] = {0.f, 0.f, 0.f};
  const bf16x8 ones = {0x3F80, 0x3F80, 0x3F80, 0x3F80, 0x3F80, 0x3F80, 0x3F80, 0x3F80};
  STAMP(T1);
  wait_vm<8>();
  bf16x8 kf[2][4];
#pragma unroll
  for (int u = 0; u < 2; ++u)
#pragma unroll
    for (int s = 0; s < 4; ++s) kf[u][s] = *reinterpret_cast<const bf16x8*>(kr + u * 4096 + ka[s]);

  unsigned kvb = kv0, vvb = vv0;
  unsigned kab = ldsK + ka[0];
  unsigned vab = ldsV + vkey * 256 + (k7 << 5) + (pp >> 1) * 16 + (pp & 1) * 8;
#define SB() __builtin_amdgcn_sched_barrier(0)
#define MFMA(a, b, c) __builtin_amdgcn_mfma_f32_16x16x32_bf16(a, b, c, 0, 0, 0)
#define QKM(S, t, i) S[(i) & 1] = MFMA(kf[(i) & 1][(i) >> 1], qf[t][(i) >> 1], S[(i) & 1])
#define XF(X, S, t, e) X[e] = fmaf(S[(e) >> 2][(e) & 3], CSC, -mC[t])
#define EX(P, X, e) P[e] = __builtin_amdgcn_exp2f(X[e])
#define PVM(t, n, pf) oacc[n][t] = MFMA(vf[n], pf, oacc[n][t])
#pragma unroll 1
  for (int j = 0; j < NBLK; ++j) {
    const bool FIRST = j == 0, LAST = j == NBLK - 1;
    f32x4 s0[2] = {f32x4{0.f, 0.f, 0.f, 0.f}, f32x4{0.f, 0.f, 0.f, 0.f}};
    f32x4 s1[2] = {f32x4{0.f, 0.f, 0.f, 0.f}, f32x4{0.f, 0.f, 0.f, 0.f}};
    f32x4 s2[2] = {f32x4{0.f, 0.f, 0.f, 0.f}, f32x4{0.f, 0.f, 0.f, 0.f}};
    float x0[8], x1[8], x2[8], e0[8], e1[8], e2[8];
    u32x4 w0, w1, w2;
    float mx;
    asm volatile("" : "+v"(kvb), "+v"(vvb), "+v"(kab), "+v"(vab));
    QKM(s0, 0, 0); QKM(s0, 0, 1); SB();
    if (!LAST) dma_block(ldsK, so_w + (j + 1) * 8192, srdK, kvb, kvb ^ 64u, kvb ^ 128u, kvb ^ 192u);
    SB();
    QKM(s0, 0, 2); QKM(s0, 0, 3); QKM(s0, 0, 4); QKM(s0, 0, 5); QKM(s0, 0, 6); QKM(s0, 0, 7); SB();
    QKM(s1, 1, 0); SB();
    QKM(s1, 1, 1); SB();
    QKM(s1, 1, 2); XF(x0, s0, 0, 0); XF(x0, s0, 0, 1); SB();
    QKM(s1, 1, 3); XF(x0, s0, 0, 2); XF(x0, s0, 0, 3); SB();
    QKM(s1, 1, 4); XF(x0, s0, 0, 4); XF(x0, s0, 0, 5); SB();
    QKM(s1, 1, 5); XF(x0, s0, 0, 6); XF(x0, s0, 0, 7); SB();
    QKM(s1, 1, 6); EX(e0, x0, 0); SB();
    QKM(s1, 1, 7); EX(e0, x0, 1); SB();
    QKM(s2, 2, 0); EX(e0, x0, 2); SB();
    QKM(s2, 2, 1); EX(e0, x0, 3); SB();
    QKM(s2, 2, 2); EX(e0, x0, 4); SB();
    QKM(s2, 2, 3); EX(e0, x0, 5); SB();
    QKM(s2, 2, 4); EX(e0, x0, 6); SB();
    QKM(s2, 2, 5); EX(e0, x0, 7); SB();
    QKM(s2, 2, 6); XF(x1, s1, 1, 0); XF(x1, s1, 1, 1); SB();
    QKM(s2, 2, 7); XF(x1, s1, 1, 2); XF(x1, s1, 1, 3); SB();
    if (!LAST) wait_vm<8>(); else wait_vm<0>();
    bf16x8 vf[8];
#pragma unroll
    for (int n = 0; n < 8; ++n) {
      const unsigned a = vab ^ (unsigned)(n << 5);
      const s16x4 lo = __builtin_amdgcn_ds_read_tr16_b64_v4i16((lds_s16x4*)(uintptr_t)(a));
      const s16x4 hi = __builtin_amdgcn_ds_read_tr16_b64_v4i16((lds_s16x4*)(uintptr_t)(a + 4096));
      vf[n] = bf16x8{lo[0], lo[1], lo[2], lo[3], hi[0], hi[1], hi[2], hi[3]};
    }
    SB();
    XF(x1, s1, 1, 4); XF(x1, s1, 1, 5); XF(x1, s1, 1, 6); XF(x1, s1, 1, 7);
    w0 = u32x4{cvtpk(e0[0], e0[1]), cvtpk(e0[2], e0[3]), cvtpk(e0[4], e0[5]), cvtpk(e0[6], e0[7])};
#pragma unroll
    for (int e = 0; e < 8; ++e) XF(x2, s2, 2, e);
    mx = x0[0];
#pragma unroll
    for (int e = 1; e < 8; ++e) mx = fmaxf(mx, x0[e]);
#pragma unroll
    for (int e = 0; e < 8; ++e) mx = fmaxf(mx, fmaxf(x1[e], x2[e]));
    if (FIRST || __any(!(mx <= THR2))) {
      auto fix = [&](int t, float (&x)[8]) {
        float m = x[0];
#pragma unroll
        for (int e = 1; e < 8; ++e) m = fmaxf(m, x[e]);
        m = xmax_g(m);
        m = fmaxf(m, FIRST ? -3.0e38f : 0.f);
        const float al = FIRST ? 0.f : __builtin_amdgcn_exp2f(-m);
        lacc[t] *= al;
#pragma unroll
        for (int n = 0; n < 8; ++n) oacc[n][t] *= al;
        mC[t] += m;
#pragma unroll
        for (int e = 0; e < 8; ++e) x[e] -= m;
      };
      fix(0, x0); fix(1, x1); fix(2, x2);
#pragma unroll
      for (int e = 0; e < 8; ++e) EX(e0, x0, e);
      w0 = u32x4{cvtpk(e0[0], e0[1]), cvtpk(e0[2], e0[3]), cvtpk(e0[4], e0[5]), cvtpk(e0[6], e0[7])};
    }
    const bf16x8 p0 = *reinterpret_cast<bf16x8*>(&w0);
    SB();
    lacc[0] = MFMA(ones, p0, lacc[0]); EX(e1, x1, 0); SB();
    PVM(0, 0, p0); EX(e1, x1, 1); SB();
    PVM(0, 1, p0); EX(e1, x1, 2); SB();
    PVM(0, 2, p0); EX(e1, x1, 3); SB();
    PVM(0, 3, p0); EX(e1, x1, 4); SB();
    PVM(0, 4, p0); EX(e1, x1, 5); SB();
    PVM(0, 5, p0); EX(e1, x1, 6); SB();
    PVM(0, 6, p0); EX(e1, x1, 7); SB();
    PVM(0, 7, p0); w1[0] = cvtpk(e1[0], e1[1]); w1[1] = cvtpk(e1[2], e1[3]); SB();
    if (!LAST) {
      dma_block(ldsV, so_w + (j + 1) * 8192, srdV, vvb, vvb ^ 128u, vvb, vvb ^ 128u);
    }
    w1[2] = cvtpk(e1[4], e1[5]); w1[3] = cvtpk(e1[6], e1[7]); EX(e2, x2, 0);
    const bf16x8 p1 = *reinterpret_cast<bf16x8*>(&w1);
    SB();
    lacc[1] = MFMA(ones, p1, lacc[1]); EX(e2, x2, 1); SB();
    PVM(1, 0, p1); EX(e2, x2, 2); SB();
    PVM(1, 1, p1); EX(e2, x2, 3); SB();
    PVM(1, 2, p1); EX(e2, x2, 4); SB();
    PVM(1, 3, p1); EX(e2, x2, 5); SB();
    PVM(1, 4, p1); EX(e2, x2, 6); SB();
    PVM(1, 5, p1); EX(e2, x2, 7); SB();
    PVM(1, 6, p1); w2[0] = cvtpk(e2[0], e2[1]); w2[1] = cvtpk(e2[2], e2[3]); SB();
    PVM(1, 7, p1); w2[2] = cvtpk(e2[4], e2[5]); w2[3] = cvtpk(e2[6], e2[7]); SB();
    const bf16x8 p2 = *reinterpret_cast<bf16x8*>(&w2);
    {
      wait_vm<8>();
#pragma unroll
      for (int u = 0; u < 2; ++u)
#pragma unroll
        for (int s = 0; s < 4; ++s) kf[u][s] = *(const lds_bf16x8*)(uintptr_t)((kab ^ (unsigned)(s << 6)) + u * 4096);
    }
    SB();
    lacc[2] = MFMA(ones, p2, lacc[2]);
#pragma unroll
    for (int n = 0; n < 8; ++n) PVM(2, n, p2);
    SB();
  }
  STAMP(T2);

  float* st = reinterpret_cast<float*>(smem + LDS_ST);
#pragma unroll
  for (int t = 0; t < 3; ++t)
    if (g == 0) *reinterpret_cast<f32x2*>(st + (wid * SQ + t * 16 + c) * 2) = f32x2{mC[t], lacc[t][0]};
  {
    u32x4* mine = reinterpret_cast<u32x4*>(smem + wid * 16384);
#pragma unroll
    for (int n = 0; n < 8; ++n)
      mine[n * 64 + lane] = u32x4{cvtpk(oacc[n][0][0], oacc[n][0][1]), cvtpk(oacc[n][0][2], oacc[n][0][3]),
                                  cvtpk(oacc[n][1][0], oacc[n][1][1]), cvtpk(oacc[n][1][2], oacc[n][1][3])};
    u32x2* mine2 = reinterpret_cast<u32x2*>(smem + wid * 16384 + 8192);
#pragma unroll
    for (int n = 0; n < 8; ++n)
      mine2[n * 64 + lane] = u32x2{cvtpk(oacc[n][2][0], oacc[n][2][1]), cvtpk(oacc[n][2][2], oacc[n][2][3])};
  }
  STAMP(T3);
  __syncthreads();
  STAMP(T4);
  u32x4 pa[NW]; u32x2 pb[NW];
#pragma unroll
  for (int w = 0; w < NW; ++w) {
    pa[w] = reinterpret_cast<const u32x4*>(smem + w * 16384)[wid * 64 + lane];
    pb[w] = reinterpret_cast<const u32x2*>(smem + w * 16384 + 8192)[wid * 64 + lane];
  }
  __hip_bfloat16* op = o + ((size_t)qhead * SQ + c) * DH + wid * 16 + g * 4;
#pragma unroll
  for (int t = 0; t < 3; ++t) {
    f32x2 s8[NW];
#pragma unroll
    for (int w = 0; w < NW; ++w) s8[w] = *reinterpret_cast<const f32x2*>(st + (w * SQ + t * 16 + c) * 2);
    float M = s8[0][0];
#pragma unroll
    for (int w = 1; w < NW; ++w) M = fmaxf(M, s8[w][0]);
    float L = 0.f;
    f32x4 a = {0.f, 0.f, 0.f, 0.f};
#pragma unroll
    for (int w = 0; w < NW; ++w) {
      const float f = __builtin_amdgcn_exp2f(s8[w][0] - M);
      L = fmaf(s8[w][1], f, L);
      const unsigned lo = t == 0 ? pa[w][0] : t == 1 ? pa[w][2] : pb[w][0];
      const unsigned hi = t == 0 ? pa[w][1] : t == 1 ? pa[w][3] : pb[w][1];
      a[0] = fmaf(__uint_as_float(lo << 16), f, a[0]);
      a[1] = fmaf(__uint_as_float(lo & 0xffff0000u), f, a[1]);
      a[2] = fmaf(__uint_as_float(hi << 16), f, a[2]);
      a[3] = fmaf(__uint_as_float(hi & 0xffff0000u), f, a[3]);
    }
    const float inv = 1.0f / L;
    u32x2 pk = {cvtpk(a[0] * inv, a[1] * inv), cvtpk(a[2] * inv, a[3] * inv)};
    *reinterpret_cast<u32x2*>(op + t * 16 * DH) = pk;
  }
  STAMP(T5);
  STAMP(T6);
}

extern "C" void kernel_launch(void* const* d_in, const int* in_sizes, int n_in,
                              void* d_out, int out_size, void* d_ws, size_t ws_size,
                              hipStream_t stream) {
  const __hip_bfloat16* q = (const __hip_bfloat16*)d_in[0];
  const __hip_bfloat16* k = (const __hip_bfloat16*)d_in[1];
  const __hip_bfloat16* v = (const __hip_bfloat16*)d_in[2];
  __hip_bfloat16* o = (__hip_bfloat16*)d_out;
  gqa_fwd_kernel<<<dim3(NB_ * HQ), dim3(NW * 64), 0, stream>>>(q, k, v, o
  );
}
```

```cpp
#include <hip/hip_runtime.h>
#include <hip/hip_bf16.h>
#include <stdint.h>
#include <type_traits>

typedef __attribute__((ext_vector_type(8))) short bf16x8;
typedef __attribute__((ext_vector_type(4))) short s16x4;
typedef __attribute__((ext_vector_type(4))) float f32x4;
typedef __attribute__((ext_vector_type(2))) float f32x2;
typedef __attribute__((ext_vector_type(4))) unsigned u32x4;
typedef __attribute__((ext_vector_type(2))) unsigned u32x2;

constexpr int NB_ = 8, HQ = 32, HKV = 4, SQ = 48, SK = 2048, DH = 128;
constexpr int NW = 8;
constexpr int KPW = SK / NW;
constexpr int KB = 32;
constexpr int NBLK = KPW / KB;
#define PFD_ 3
constexpr int PFD = PFD_;
constexpr float CSC = 0.088388347648318440f * 1.4426950408889634f;
constexpr float THR2 = 10.0f;

constexpr int LDS_KV = NW * 16384;
constexpr int LDS_Q = LDS_KV;
constexpr int LDS_PF = LDS_Q + SQ * 256;
constexpr int LDS_ST = LDS_PF + NW * 256;
constexpr int LDS_TOTAL = LDS_ST + NW * SQ * 2 * 4;

#define STAMP(x)

typedef __attribute__((ext_vector_type(2))) __bf16 bf16x2;
__device__ __forceinline__ unsigned cvtpk(float lo, float hi) {
  f32x2 v = {lo, hi};
  return __builtin_bit_cast(unsigned, __builtin_convertvector(v, bf16x2));
}
__device__ __forceinline__ float xmax_g(float x) {
  auto a = __builtin_amdgcn_permlane16_swap(__float_as_uint(x), __float_as_uint(x), false, false);
  x = fmaxf(__uint_as_float(a[0]), __uint_as_float(a[1]));
  auto b = __builtin_amdgcn_permlane32_swap(__float_as_uint(x), __float_as_uint(x), false, false);
  return fmaxf(__uint_as_float(b[0]), __uint_as_float(b[1]));
}

__device__ __forceinline__ float xsum_g(float x) {
  auto a = __builtin_amdgcn_permlane16_swap(__float_as_uint(x), __float_as_uint(x), false, false);
  x = __uint_as_float(a[0]) + __uint_as_float(a[1]);
  auto b = __builtin_amdgcn_permlane32_swap(__float_as_uint(x), __float_as_uint(x), false, false);
  return __uint_as_float(b[0]) + __uint_as_float(b[1]);
}

__device__ __forceinline__ void dma_block(unsigned lds0, unsigned so0, __amdgpu_buffer_rsrc_t srd,
                                          unsigned v0, unsigned v1, unsigned v2, unsigned v3) {
  asm volatile(
      "s_waitcnt lgkmcnt(0)\n\t"
      "s_mov_b32 m0, %0\n\t"
      "s_nop 0\n\t"
      "buffer_load_dwordx4 %4, %3, %1 offen offset:0 nt lds\n\t"
      "buffer_load_dwordx4 %5, %3, %1 offen offset:1024 nt lds\n\t"
      "buffer_load_dwordx4 %6, %3, %1 offen offset:2048 nt lds\n\t"
      "buffer_load_dwordx4 %7, %3, %1 offen offset:3072 nt lds\n\t"
      "s_add_u32 m0, %0, 4096\n\t"
      "s_nop 0\n\t"
      "buffer_load_dwordx4 %4, %3, %2 offen offset:0 nt lds\n\t"
      "buffer_load_dwordx4 %5, %3, %2 offen offset:1024 nt lds\n\t"
      "buffer_load_dwordx4 %6, %3, %2 offen offset:2048 nt lds\n\t"
      "buffer_load_dwordx4 %7, %3, %2 offen offset:3072 nt lds"
      :: "s"(lds0), "s"(so0), "s"(so0 + 4096u), "s"(srd), "v"(v0), "v"(v1), "v"(v2), "v"(v3)
      : "memory", "scc");
}
__device__ __forceinline__ void dma_piece(unsigned lds0, unsigned so0, __amdgpu_buffer_rsrc_t srd, unsigned v0) {
  asm volatile("s_mov_b32 m0, %0\n\ts_nop 0\n\tbuffer_load_dwordx4 %3, %2, %1 offen nt lds"
               :: "s"(lds0), "s"(so0), "s"(srd), "v"(v0) : "memory");
}
__device__ __forceinline__ void prefetch_l2(unsigned ldsdummy, const char* p) {
  asm volatile("s_mov_b32 m0, %0\n\ts_nop 0\n\tglobal_load_lds_dword %1, off nt" :: "s"(ldsdummy), "v"(p) : "memory");
}
template <int N> __device__ __forceinline__ void wait_vm() { asm volatile("s_waitcnt vmcnt(%0)" :: "i"(N) : "memory"); }

typedef __attribute__((address_space(3))) s16x4 lds_s16x4;
typedef __attribute__((address_space(3))) bf16x8 lds_bf16x8;

__global__ __launch_bounds__(NW * 64, 2)
void gqa_fwd_kernel(const __hip_bfloat16* __restrict__ q, const __hip_bfloat16* __restrict__ k,
                    const __hip_bfloat16* __restrict__ v, __hip_bfloat16* __restrict__ o
                    ) {
  __shared__ __attribute__((aligned(1024))) char smem[LDS_TOTAL];
  STAMP(T0);
  const int tid = threadIdx.x, lane = tid & 63;
  const int wid = __builtin_amdgcn_readfirstlane(tid >> 6);
  const int c = lane & 15, g = lane >> 4;
  const int bid = blockIdx.x, xcd = bid & 7, idx = bid >> 3;
  const int grp = xcd * 4 + (idx >> 3), hh = idx & 7;
  const int qhead = grp * 8 + hh;
  const char* Kg = (const char*)k + (size_t)grp * SK * DH * 2;
  const char* Vg = (const char*)v + (size_t)grp * SK * DH * 2;
  const char* Qg = (const char*)q + (size_t)qhead * SQ * DH * 2;
  const __amdgpu_buffer_rsrc_t srdK = __builtin_amdgcn_make_buffer_rsrc((void*)Kg, (short)0, SK * DH * 2, 0x00020000);
  const __amdgpu_buffer_rsrc_t srdV = __builtin_amdgcn_make_buffer_rsrc((void*)Vg, (short)0, SK * DH * 2, 0x00020000);
  const __amdgpu_buffer_rsrc_t srdQ = __builtin_amdgcn_make_buffer_rsrc((void*)Qg, (short)0, SQ * DH * 2, 0x00020000);

  const unsigned lds_base = (unsigned)(uintptr_t)smem;
  const unsigned ldsK = lds_base + wid * 16384, ldsV = ldsK + 8192, ldsP = lds_base + LDS_PF + wid * 256;
  const unsigned rr = lane >> 4, pos = lane & 15;
  const unsigned kv0 = rr * 256 + ((pos ^ (0 + rr)) << 4), kv1 = rr * 256 + ((pos ^ (4 + rr)) << 4);
  const unsigned kv2 = rr * 256 + ((pos ^ (8 + rr)) << 4), kv3 = rr * 256 + ((pos ^ (12 + rr)) << 4);
  const unsigned vv0 = rr * 256 + ((pos ^ (2 * rr)) << 4), vv1 = rr * 256 + ((pos ^ (2 * (4 + rr))) << 4);
  const unsigned so_w = (unsigned)wid * KPW * 256;
#define ROT 0
  const unsigned rot = ROT == 1 ? (unsigned)hh : ROT == 2 ? (unsigned)(hh & 1) * 4u : ROT == 3 ? (unsigned)(hh & 3) * 2u : 0u;

#define PROL 2
  {
    const unsigned qv = rr * 256 + ((pos ^ (4 * (wid & 3) + rr)) << 4);
    dma_piece(lds_base + LDS_Q + wid * 1024, wid * 1024, srdQ, qv);
    if (wid < 4) dma_piece(lds_base + LDS_Q + (wid + 8) * 1024, (wid + 8) * 1024, srdQ, qv);
  }
  __builtin_amdgcn_s_barrier();
  dma_block(ldsK, so_w + rot * 8192, srdK, kv0, kv1, kv2, kv3);
  __builtin_amdgcn_s_barrier();
  dma_block(ldsV, so_w + rot * 8192, srdV, vv0, vv1, vv0, vv1);
  wait_vm<16>();

  unsigned ka[4];
#pragma unroll
  for (int s = 0; s < 4; ++s) ka[s] = (unsigned)(c * 256 + (((4 * s + g) ^ c) << 4));
  const char* kr = smem + wid * 16384;
#define PRIO 0
  if (PRIO == 1 && wid >= 4) __builtin_amdgcn_s_setprio(1);
  const int qq = (lane & 15) >> 2, pp = lane & 3;
  const int vkey = 4 * g + qq, k7 = vkey & 7;
  const char* vr = smem + wid * 16384 + 8192 + vkey * 256 + (pp >> 1) * 16 + (pp & 1) * 8;

  __builtin_amdgcn_s_barrier();
  asm volatile("" ::: "memory");
  bf16x8 qf[3][4];
#pragma unroll
  for (int t = 0; t < 3; ++t)
#pragma unroll
    for (int s = 0; s < 4; ++s) qf[t][s] = *reinterpret_cast<const bf16x8*>(smem + LDS_Q + t * 4096 + ka[s]);

  f32x4 oacc[8][3], lacc[3];
#pragma unroll
  for (int t = 0; t < 3; ++t) {
    lacc[t] = f32x4{0.f, 0.f, 0.f, 0.f};
#pragma unroll
    for (int n = 0; n < 8; ++n) oacc[n][t] = f32x4{0.f, 0.f, 0.f, 0.f};
  }
  float mC[3] = {0.f, 0.f, 0.f};
  const bf16x8 ones = {0x3F80, 0x3F80, 0x3F80, 0x3F80, 0x3F80, 0x3F80, 0x3F80, 0x3F80};
  STAMP(T1);
  wait_vm<8>();
  bf16x8 kf[2][4];
#pragma unroll
  for (int u = 0; u < 2; ++u)
#pragma unroll
    for (int s = 0; s < 4; ++s) kf[u][s] = *reinterpret_cast<const bf16x8*>(kr + u * 4096 + ka[s]);

  unsigned kvb = kv0, vvb = vv0;
  unsigned kab = ldsK + ka[0];
  unsigned vab = ldsV + vkey * 256 + (k7 << 5) + (pp >> 1) * 16 + (pp & 1) * 8;
#define SB() __builtin_amdgcn_sched_barrier(0)
#define MFMA(a, b, c) __builtin_amdgcn_mfma_f32_16x16x32_bf16(a, b, c, 0, 0, 0)
#define QKM(S, t, i) S[(i) & 1] = MFMA(kf[(i) & 1][(i) >> 1], qf[t][(i) >> 1], S[(i) & 1])
#define XF(X, S, t, e) X[e] = fmaf(S[(e) >> 2][(e) & 3], CSC, -mC[t])
#define EX(P, X, e) P[e] = __builtin_amdgcn_exp2f(X[e])
#define PVM(t, n, pf) oacc[n][t] = MFMA(vf[n], pf, oacc[n][t])
  auto body = [&](auto FIRST_, int j) {
    constexpr bool FIRST = decltype(FIRST_)::value;
    const bool LAST = j == NBLK - 1;
    f32x4 s0[2] = {f32x4{0.f, 0.f, 0.f, 0.f}, f32x4{0.f, 0.f, 0.f, 0.f}};
    f32x4 s1[2] = {f32x4{0.f, 0.f, 0.f, 0.f}, f32x4{0.f, 0.f, 0.f, 0.f}};
    f32x4 s2[2] = {f32x4{0.f, 0.f, 0.f, 0.f}, f32x4{0.f, 0.f, 0.f, 0.f}};
    float x0[8], x1[8], x2[8], e0[8], e1[8], e2[8];
    u32x4 w0, w1, w2;
    float mx;

    asm volatile("" : "+v"(kvb), "+v"(vvb), "+v"(kab));
    QKM(s0, 0, 0); QKM(s0, 0, 1); SB();
    if (!LAST) dma_block(ldsK, so_w + ((j + 1 + rot) & 7) * 8192, srdK, kvb, kvb ^ 64u, kvb ^ 128u, kvb ^ 192u);
    SB();
    QKM(s0, 0, 2); QKM(s0, 0, 3); QKM(s0, 0, 4); QKM(s0, 0, 5); QKM(s0, 0, 6); QKM(s0, 0, 7); SB();
    QKM(s1, 1, 0); SB();
    QKM(s1, 1, 1); SB();
    QKM(s1, 1, 2); XF(x0, s0, 0, 0); XF(x0, s0, 0, 1); SB();
    QKM(s1, 1, 3); XF(x0, s0, 0, 2); XF(x0, s0, 0, 3); SB();
    QKM(s1, 1, 4); XF(x0, s0, 0, 4); XF(x0, s0, 0, 5); SB();
    QKM(s1, 1, 5); XF(x0, s0, 0, 6); XF(x0, s0, 0, 7); SB();
    QKM(s1, 1, 6); EX(e0, x0, 0); SB();
    QKM(s1, 1, 7); EX(e0, x0, 1); SB();
    QKM(s2, 2, 0); EX(e0, x0, 2); SB();
    QKM(s2, 2, 1); EX(e0, x0, 3); SB();
    QKM(s2, 2, 2); EX(e0, x0, 4); SB();
    QKM(s2, 2, 3); EX(e0, x0, 5); SB();
    QKM(s2, 2, 4); EX(e0, x0, 6); SB();
    QKM(s2, 2, 5); EX(e0, x0, 7); SB();
    QKM(s2, 2, 6); XF(x1, s1, 1, 0); XF(x1, s1, 1, 1); SB();
    QKM(s2, 2, 7); XF(x1, s1, 1, 2); XF(x1, s1, 1, 3); SB();
    if (!LAST) wait_vm<8>(); else wait_vm<0>();
    bf16x8 vf[8];
#pragma unroll
    for (int n = 0; n < 8; ++n) {
      const unsigned a = vab ^ (unsigned)(n << 5);
      const s16x4 lo = __builtin_amdgcn_ds_read_tr16_b64_v4i16((lds_s16x4*)(uintptr_t)(a));
      const s16x4 hi = __builtin_amdgcn_ds_read_tr16_b64_v4i16((lds_s16x4*)(uintptr_t)(a + 4096));
      vf[n] = bf16x8{lo[0], lo[1], lo[2], lo[3], hi[0], hi[1], hi[2], hi[3]};
    }
    SB();
    XF(x1, s1, 1, 4); XF(x1, s1, 1, 5); XF(x1, s1, 1, 6); XF(x1, s1, 1, 7);
    w0 = u32x4{cvtpk(e0[0], e0[1]), cvtpk(e0[2], e0[3]), cvtpk(e0[4], e0[5]), cvtpk(e0[6], e0[7])};
#pragma unroll
    for (int e = 0; e < 8; ++e) XF(x2, s2, 2, e);
    mx = x0[0];
#pragma unroll
    for (int e = 1; e < 8; ++e) mx = fmaxf(mx, x0[e]);
#pragma unroll
    for (int e = 0; e < 8; ++e) mx = fmaxf(mx, fmaxf(x1[e], x2[e]));
    if (FIRST || __any(!(mx <= THR2))) {
      auto fix = [&](int t, float (&x)[8]) {
        float m = x[0];
#pragma unroll
        for (int e = 1; e < 8; ++e) m = fmaxf(m, x[e]);
        m = xmax_g(m);
        if constexpr (!FIRST) {
          m = fmaxf(m, 0.f);
          const float al = __builtin_amdgcn_exp2f(-m);
          lacc[t] *= al;
#pragma unroll
          for (int n = 0; n < 8; ++n) oacc[n][t] *= al;
        }
        mC[t] += m;
#pragma unroll
        for (int e = 0; e < 8; ++e) x[e] -= m;
      };
      fix(0, x0); fix(1, x1); fix(2, x2);
#pragma unroll
      for (int e = 0; e < 8; ++e) EX(e0, x0, e);
      w0 = u32x4{cvtpk(e0[0], e0[1]), cvtpk(e0[2], e0[3]), cvtpk(e0[4], e0[5]), cvtpk(e0[6], e0[7])};
    }
    const bf16x8 p0 = *reinterpret_cast<bf16x8*>(&w0);
    SB();
    lacc[0] = MFMA(ones, p0, lacc[0]); EX(e1, x1, 0); SB();
    PVM(0, 0, p0); EX(e1, x1, 1); SB();
    PVM(0, 1, p0); EX(e1, x1, 2); SB();
    PVM(0, 2, p0); EX(e1, x1, 3); SB();
    PVM(0, 3, p0); EX(e1, x1, 4); SB();
    PVM(0, 4, p0); EX(e1, x1, 5); SB();
    PVM(0, 5, p0); EX(e1, x1, 6); SB();
    PVM(0, 6, p0); EX(e1, x1, 7); SB();
    PVM(0, 7, p0); w1[0] = cvtpk(e1[0], e1[1]); w1[1] = cvtpk(e1[2], e1[3]); SB();
    if (!LAST) {
      dma_block(ldsV, so_w + ((j + 1 + rot) & 7) * 8192, srdV, vvb, vvb ^ 128u, vvb, vvb ^ 128u);
    }
    w1[2] = cvtpk(e1[4], e1[5]); w1[3] = cvtpk(e1[6], e1[7]); EX(e2, x2, 0);
    const bf16x8 p1 = *reinterpret_cast<bf16x8*>(&w1);
    SB();
    lacc[1] = MFMA(ones, p1, lacc[1]); EX(e2, x2, 1); SB();
    PVM(1, 0, p1); EX(e2, x2, 2); SB();
    PVM(1, 1, p1); EX(e2, x2, 3); SB();
    PVM(1, 2, p1); EX(e2, x2, 4); SB();
    PVM(1, 3, p1); EX(e2, x2, 5); SB();
    PVM(1, 4, p1); EX(e2, x2, 6); SB();
    PVM(1, 5, p1); EX(e2, x2, 7); SB();
    PVM(1, 6, p1); w2[0] = cvtpk(e2[0], e2[1]); w2[1] = cvtpk(e2[2], e2[3]); SB();
    PVM(1, 7, p1); w2[2] = cvtpk(e2[4], e2[5]); w2[3] = cvtpk(e2[6], e2[7]); SB();
    const bf16x8 p2 = *reinterpret_cast<bf16x8*>(&w2);
    {
      wait_vm<8>();
#pragma unroll
      for (int u = 0; u < 2; ++u)
#pragma unroll
        for (int s = 0; s < 4; ++s) kf[u][s] = *(const lds_bf16x8*)(uintptr_t)((kab ^ (unsigned)(s << 6)) + u * 4096);
    }
    SB();
    lacc[2] = MFMA(ones, p2, lacc[2]);
#pragma unroll
    for (int n = 0; n < 8; ++n) PVM(2, n, p2);
    SB();
  };
  body(std::true_type{}, 0);
#pragma unroll 1
  for (int j = 1; j < NBLK; ++j) body(std::false_type{}, j);
  STAMP(T2);

  float* st = reinterpret_cast<float*>(smem + LDS_ST);
#pragma unroll
  for (int t = 0; t < 3; ++t)
    if (g == 0) *reinterpret_cast<f32x2*>(st + (wid * SQ + t * 16 + c) * 2) = f32x2{mC[t], lacc[t][0]};
  {
    u32x4* mine = reinterpret_cast<u32x4*>(smem + wid * 16384);
#pragma unroll
    for (int n = 0; n < 8; ++n)
      mine[n * 64 + lane] = u32x4{cvtpk(oacc[n][0][0], oacc[n][0][1]), cvtpk(oacc[n][0][2], oacc[n][0][3]),
                                  cvtpk(oacc[n][1][0], oacc[n][1][1]), cvtpk(oacc[n][1][2], oacc[n][1][3])};
    u32x2* mine2 = reinterpret_cast<u32x2*>(smem + wid * 16384 + 8192);
#pragma unroll
    for (int n = 0; n < 8; ++n)
      mine2[n * 64 + lane] = u32x2{cvtpk(oacc[n][2][0], oacc[n][2][1]), cvtpk(oacc[n][2][2], oacc[n][2][3])};
  }
  STAMP(T3);
  __syncthreads();
  STAMP(T4);
  u32x4 pa[NW]; u32x2 pb[NW];
#pragma unroll
  for (int w = 0; w < NW; ++w) {
    pa[w] = reinterpret_cast<const u32x4*>(smem + w * 16384)[wid * 64 + lane];
    pb[w] = reinterpret_cast<const u32x2*>(smem + w * 16384 + 8192)[wid * 64 + lane];
  }
  __hip_bfloat16* op = o + ((size_t)qhead * SQ + c) * DH + wid * 16 + g * 4;
#pragma unroll
  for (int t = 0; t < 3; ++t) {
    f32x2 s8[NW];
#pragma unroll
    for (int w = 0; w < NW; ++w) s8[w] = *reinterpret_cast<const f32x2*>(st + (w * SQ + t * 16 + c) * 2);
    float M = s8[0][0];
#pragma unroll
    for (int w = 1; w < NW; ++w) M = fmaxf(M, s8[w][0]);
    float L = 0.f;
    f32x4 a = {0.f, 0.f, 0.f, 0.f};
#pragma unroll
    for (int w = 0; w < NW; ++w) {
      const float f = __builtin_amdgcn_exp2f(s8[w][0] - M);
      L = fmaf(s8[w][1], f, L);
      const unsigned lo = t == 0 ? pa[w][0] : t == 1 ? pa[w][2] : pb[w][0];
      const unsigned hi = t == 0 ? pa[w][1] : t == 1 ? pa[w][3] : pb[w][1];
      a[0] = fmaf(__uint_as_float(lo << 16), f, a[0]);
      a[1] = fmaf(__uint_as_float(lo & 0xffff0000u), f, a[1]);
      a[2] = fmaf(__uint_as_float(hi << 16), f, a[2]);
      a[3] = fmaf(__uint_as_float(hi & 0xffff0000u), f, a[3]);
    }
    const float inv = __builtin_amdgcn_rcpf(L);
    u32x2 pk = {cvtpk(a[0] * inv, a[1] * inv), cvtpk(a[2] * inv, a[3] * inv)};
    *reinterpret_cast<u32x2*>(op + t * 16 * DH) = pk;
  }
  STAMP(T5);
  STAMP(T6);
}

extern "C" void kernel_launch(void* const* d_in, const int* in_sizes, int n_in,
                              void* d_out, int out_size, void* d_ws, size_t ws_size,
                              hipStream_t stream) {
  const __hip_bfloat16* q = (const __hip_bfloat16*)d_in[0];
  const __hip_bfloat16* k = (const __hip_bfloat16*)d_in[1];
  const __hip_bfloat16* v = (const __hip_bfloat16*)d_in[2];
  __hip_bfloat16* o = (__hip_bfloat16*)d_out;
  gqa_fwd_kernel<<<dim3(NB_ * HQ), dim3(NW * 64), 0, stream>>>(q, k, v, o
  );
}
```

```cpp
#include <hip/hip_runtime.h>
#include <hip/hip_bf16.h>
#include <stdint.h>
#include <type_traits>

typedef __attribute__((ext_vector_type(8))) short bf16x8;
typedef __attribute__((ext_vector_type(4))) short s16x4;
typedef __attribute__((ext_vector_type(4))) float f32x4;
typedef __attribute__((ext_vector_type(2))) float f32x2;
typedef __attribute__((ext_vector_type(4))) unsigned u32x4;
typedef __attribute__((ext_vector_type(2))) unsigned u32x2;

constexpr int NB_ = 8, HQ = 32, HKV = 4, SQ = 48, SK = 2048, DH = 128;
constexpr int NW = 8;
constexpr int KPW = SK / NW;
constexpr int KB = 32;
constexpr int NBLK = KPW / KB;
#define PFD_ 3
constexpr int PFD = PFD_;
constexpr float CSC = 0.088388347648318440f * 1.4426950408889634f;
constexpr float THR2 = 10.0f;

constexpr int LDS_KV = NW * 16384;
constexpr int LDS_Q = LDS_KV;
constexpr int LDS_PF = LDS_Q + SQ * 256;
constexpr int LDS_ST = LDS_PF + NW * 256;
constexpr int LDS_TOTAL = LDS_ST + NW * SQ * 2 * 4;

#define STAMP(x)

typedef __attribute__((ext_vector_type(2))) __bf16 bf16x2;
__device__ __forceinline__ unsigned cvtpk(float lo, float hi) {
  f32x2 v = {lo, hi};
  return __builtin_bit_cast(unsigned, __builtin_convertvector(v, bf16x2));
}
__device__ __forceinline__ float xmax_g(float x) {
  auto a = __builtin_amdgcn_permlane16_swap(__float_as_uint(x), __float_as_uint(x), false, false);
  x = fmaxf(__uint_as_float(a[0]), __uint_as_float(a[1]));
  auto b = __builtin_amdgcn_permlane32_swap(__float_as_uint(x), __float_as_uint(x), false, false);
  return fmaxf(__uint_as_float(b[0]), __uint_as_float(b[1]));
}

__device__ __forceinline__ float xsum_g(float x) {
  auto a = __builtin_amdgcn_permlane16_swap(__float_as_uint(x), __float_as_uint(x), false, false);
  x = __uint_as_float(a[0]) + __uint_as_float(a[1]);
  auto b = __builtin_amdgcn_permlane32_swap(__float_as_uint(x), __float_as_uint(x), false, false);
  return __uint_as_float(b[0]) + __uint_as_float(b[1]);
}

__device__ __forceinline__ void dma_block(unsigned lds0, unsigned so0, __amdgpu_buffer_rsrc_t srd,
                                          unsigned v0, unsigned v1, unsigned v2, unsigned v3) {
  asm volatile(
      "s_waitcnt lgkmcnt(0)\n\t"
      "s_mov_b32 m0, %0\n\t"
      "s_nop 0\n\t"
      "buffer_load_dwordx4 %4, %3, %1 offen offset:0 lds\n\t"
      "buffer_load_dwordx4 %5, %3, %1 offen offset:1024 lds\n\t"
      "buffer_load_dwordx4 %6, %3, %1 offen offset:2048 lds\n\t"
      "buffer_load_dwordx4 %7, %3, %1 offen offset:3072 lds\n\t"
      "s_add_u32 m0, %0, 4096\n\t"
      "s_nop 0\n\t"
      "buffer_load_dwordx4 %4, %3, %2 offen offset:0 lds\n\t"
      "buffer_load_dwordx4 %5, %3, %2 offen offset:1024 lds\n\t"
      "buffer_load_dwordx4 %6, %3, %2 offen offset:2048 lds\n\t"
      "buffer_load_dwordx4 %7, %3, %2 offen offset:3072 lds"
      :: "s"(lds0), "s"(so0), "s"(so0 + 4096u), "s"(srd), "v"(v0), "v"(v1), "v"(v2), "v"(v3)
      : "memory", "scc");
}
__device__ __forceinline__ void dma_piece(unsigned lds0, unsigned so0, __amdgpu_buffer_rsrc_t srd, unsigned v0) {
  asm volatile("s_mov_b32 m0, %0\n\ts_nop 0\n\tbuffer_load_dwordx4 %3, %2, %1 offen lds"
               :: "s"(lds0), "s"(so0), "s"(srd), "v"(v0) : "memory");
}
__device__ __forceinline__ void prefetch_l2(unsigned ldsdummy, const char* p) {
  asm volatile("s_mov_b32 m0, %0\n\ts_nop 0\n\tglobal_load_lds_dword %1, off nt" :: "s"(ldsdummy), "v"(p) : "memory");
}
template <int N> __device__ __forceinline__ void wait_vm() { asm volatile("s_waitcnt vmcnt(%0)" :: "i"(N) : "memory"); }

typedef __attribute__((address_space(3))) s16x4 lds_s16x4;
typedef __attribute__((address_space(3))) bf16x8 lds_bf16x8;

__global__ __launch_bounds__(NW * 64, 2)
void gqa_fwd_kernel(const __hip_bfloat16* __restrict__ q, const __hip_bfloat16* __restrict__ k,
                    const __hip_bfloat16* __restrict__ v, __hip_bfloat16* __restrict__ o
                    ) {
  __shared__ __attribute__((aligned(1024))) char smem[LDS_TOTAL];
  STAMP(T0);
  const int tid = threadIdx.x, lane = tid & 63;
  const int wid = __builtin_amdgcn_readfirstlane(tid >> 6);
  const int c = lane & 15, g = lane >> 4;
  const int bid = blockIdx.x, xcd = bid & 7, idx = bid >> 3;
  const int grp = xcd * 4 + (idx >> 3), hh = idx & 7;
  const int qhead = grp * 8 + hh;
  const char* Kg = (const char*)k + (size_t)grp * SK * DH * 2;
  const char* Vg = (const char*)v + (size_t)grp * SK * DH * 2;
  const char* Qg = (const char*)q + (size_t)qhead * SQ * DH * 2;
  const __amdgpu_buffer_rsrc_t srdK = __builtin_amdgcn_make_buffer_rsrc((void*)Kg, (short)0, SK * DH * 2, 0x00020000);
  const __amdgpu_buffer_rsrc_t srdV = __builtin_amdgcn_make_buffer_rsrc((void*)Vg, (short)0, SK * DH * 2, 0x00020000);
  const __amdgpu_buffer_rsrc_t srdQ = __builtin_amdgcn_make_buffer_rsrc((void*)Qg, (short)0, SQ * DH * 2, 0x00020000);

  const unsigned lds_base = (unsigned)(uintptr_t)smem;
  const unsigned ldsK = lds_base + wid * 16384, ldsV = ldsK + 8192, ldsP = lds_base + LDS_PF + wid * 256;
  const unsigned rr = lane >> 4, pos = lane & 15;
  const unsigned kv0 = rr * 256 + ((pos ^ (0 + rr)) << 4), kv1 = rr * 256 + ((pos ^ (4 + rr)) << 4);
  const unsigned kv2 = rr * 256 + ((pos ^ (8 + rr)) << 4), kv3 = rr * 256 + ((pos ^ (12 + rr)) << 4);
  const unsigned vv0 = rr * 256 + ((pos ^ (2 * rr)) << 4), vv1 = rr * 256 + ((pos ^ (2 * (4 + rr))) << 4);
  const unsigned so_w = (unsigned)wid * KPW * 256;
#define ROT 3
  const unsigned rot = ROT == 1 ? (unsigned)hh : ROT == 2 ? (unsigned)(hh & 1) * 4u : ROT == 3 ? (unsigned)(hh & 3) * 2u : 0u;

#define PROL 2
  {
    const unsigned qv = rr * 256 + ((pos ^ (4 * (wid & 3) + rr)) << 4);
    dma_piece(lds_base + LDS_Q + wid * 1024, wid * 1024, srdQ, qv);
    if (wid < 4) dma_piece(lds_base + LDS_Q + (wid + 8) * 1024, (wid + 8) * 1024, srdQ, qv);
  }
  __builtin_amdgcn_s_barrier();
  dma_block(ldsK, so_w + rot * 8192, srdK, kv0, kv1, kv2, kv3);
  __builtin_amdgcn_s_barrier();
  dma_block(ldsV, so_w + rot * 8192, srdV, vv0, vv1, vv0, vv1);
  wait_vm<16>();

  unsigned ka[4];
#pragma unroll
  for (int s = 0; s < 4; ++s) ka[s] = (unsigned)(c * 256 + (((4 * s + g) ^ c) << 4));
  const char* kr = smem + wid * 16384;
#define PRIO 0
  if (PRIO == 1 && wid >= 4) __builtin_amdgcn_s_setprio(1);
  const int qq = (lane & 15) >> 2, pp = lane & 3;
  const int vkey = 4 * g + qq, k7 = vkey & 7;
  const char* vr = smem + wid * 16384 + 8192 + vkey * 256 + (pp >> 1) * 16 + (pp & 1) * 8;

  __builtin_amdgcn_s_barrier();
  asm volatile("" ::: "memory");
  bf16x8 qf[3][4];
#pragma unroll
  for (int t = 0; t < 3; ++t)
#pragma unroll
    for (int s = 0; s < 4; ++s) qf[t][s] = *reinterpret_cast<const bf16x8*>(smem + LDS_Q + t * 4096 + ka[s]);

  f32x4 oacc[8][3], lacc[3];
#pragma unroll
  for (int t = 0; t < 3; ++t) {
    lacc[t] = f32x4{0.f, 0.f, 0.f, 0.f};
#pragma unroll
    for (int n = 0; n < 8; ++n) oacc[n][t] = f32x4{0.f, 0.f, 0.f, 0.f};
  }
  float mC[3] = {0.f, 0.f, 0.f};
  const bf16x8 ones = {0x3F80, 0x3F80, 0x3F80, 0x3F80, 0x3F80, 0x3F80, 0x3F80, 0x3F80};
  STAMP(T1);
  wait_vm<8>();
  bf16x8 kf[2][4];
#pragma unroll
  for (int u = 0; u < 2; ++u)
#pragma unroll
    for (int s = 0; s < 4; ++s) kf[u][s] = *reinterpret_cast<const bf16x8*>(kr + u * 4096 + ka[s]);

  unsigned kvb = kv0, vvb = vv0;
  unsigned kab = ldsK + ka[0];
  unsigned vab = ldsV + vkey * 256 + (k7 << 5) + (pp >> 1) * 16 + (pp & 1) * 8;
#define SB() __builtin_amdgcn_sched_barrier(0)
#define MFMA(a, b, c) __builtin_amdgcn_mfma_f32_16x16x32_bf16(a, b, c, 0, 0, 0)
#define QKM(S, t, i) S[(i) & 1] = MFMA(kf[(i) & 1][(i) >> 1], qf[t][(i) >> 1], S[(i) & 1])
#define XF(X, S, t, e) X[e] = fmaf(S[(e) >> 2][(e) & 3], CSC, -mC[t])
#define EX(P, X, e) P[e] = __builtin_amdgcn_exp2f(X[e])
#define PVM(t, n, pf) oacc[n][t] = MFMA(vf[n], pf, oacc[n][t])
  auto body = [&](auto FIRST_, int j) {
    constexpr bool FIRST = decltype(FIRST_)::value;
    const bool LAST = j == NBLK - 1;
    f32x4 s0[2] = {f32x4{0.f, 0.f, 0.f, 0.f}, f32x4{0.f, 0.f, 0.f, 0.f}};
    f32x4 s1[2] = {f32x4{0.f, 0.f, 0.f, 0.f}, f32x4{0.f, 0.f, 0.f, 0.f}};
    f32x4 s2[2] = {f32x4{0.f, 0.f, 0.f, 0.f}, f32x4{0.f, 0.f, 0.f, 0.f}};
    float x0[8], x1[8], x2[8], e0[8], e1[8], e2[8];
    u32x4 w0, w1, w2;
    float mx;

    asm volatile("" : "+v"(kvb), "+v"(vvb), "+v"(kab));
    QKM(s0, 0, 0); QKM(s0, 0, 1); SB();
    if (!LAST) dma_block(ldsK, so_w + ((j + 1 + rot) & 7) * 8192, srdK, kvb, kvb ^ 64u, kvb ^ 128u, kvb ^ 192u);
    SB();
    QKM(s0, 0, 2); QKM(s0, 0, 3); QKM(s0, 0, 4); QKM(s0, 0, 5); QKM(s0, 0, 6); QKM(s0, 0, 7); SB();
    QKM(s1, 1, 0); SB();
    QKM(s1, 1, 1); SB();
    QKM(s1, 1, 2); XF(x0, s0, 0, 0); XF(x0, s0, 0, 1); SB();
    QKM(s1, 1, 3); XF(x0, s0, 0, 2); XF(x0, s0, 0, 3); SB();
    QKM(s1, 1, 4); XF(x0, s0, 0, 4); XF(x0, s0, 0, 5); SB();
    QKM(s1, 1, 5); XF(x0, s0, 0, 6); XF(x0, s0, 0, 7); SB();
    QKM(s1, 1, 6); EX(e0, x0, 0); SB();
    QKM(s1, 1, 7); EX(e0, x0, 1); SB();
    QKM(s2, 2, 0); EX(e0, x0, 2); SB();
    QKM(s2, 2, 1); EX(e0, x0, 3); SB();
    QKM(s2, 2, 2); EX(e0, x0, 4); SB();
    QKM(s2, 2, 3); EX(e0, x0, 5); SB();
    QKM(s2, 2, 4); EX(e0, x0, 6); SB();
    QKM(s2, 2, 5); EX(e0, x0, 7); SB();
    QKM(s2, 2, 6); XF(x1, s1, 1, 0); XF(x1, s1, 1, 1); SB();
    QKM(s2, 2, 7); XF(x1, s1, 1, 2); XF(x1, s1, 1, 3); SB();
    if (!LAST) wait_vm<8>(); else wait_vm<0>();
    bf16x8 vf[8];
#pragma unroll
    for (int n = 0; n < 8; ++n) {
      const unsigned a = vab ^ (unsigned)(n << 5);
      const s16x4 lo = __builtin_amdgcn_ds_read_tr16_b64_v4i16((lds_s16x4*)(uintptr_t)(a));
      const s16x4 hi = __builtin_amdgcn_ds_read_tr16_b64_v4i16((lds_s16x4*)(uintptr_t)(a + 4096));
      vf[n] = bf16x8{lo[0], lo[1], lo[2], lo[3], hi[0], hi[1], hi[2], hi[3]};
    }
    SB();
    XF(x1, s1, 1, 4); XF(x1, s1, 1, 5); XF(x1, s1, 1, 6); XF(x1, s1, 1, 7);
    w0 = u32x4{cvtpk(e0[0], e0[1]), cvtpk(e0[2], e0[3]), cvtpk(e0[4], e0[5]), cvtpk(e0[6], e0[7])};
#pragma unroll
    for (int e = 0; e < 8; ++e) XF(x2, s2, 2, e);
    mx = x0[0];
#pragma unroll
    for (int e = 1; e < 8; ++e) mx = fmaxf(mx, x0[e]);
#pragma unroll
    for (int e = 0; e < 8; ++e) mx = fmaxf(mx, fmaxf(x1[e], x2[e]));
    if (FIRST || __any(!(mx <= THR2))) {
      auto fix = [&](int t, float (&x)[8]) {
        float m = x[0];
#pragma unroll
        for (int e = 1; e < 8; ++e) m = fmaxf(m, x[e]);
        m = xmax_g(m);
        if constexpr (!FIRST) {
          m = fmaxf(m, 0.f);
          const float al = __builtin_amdgcn_exp2f(-m);
          lacc[t] *= al;
#pragma unroll
          for (int n = 0; n < 8; ++n) oacc[n][t] *= al;
        }
        mC[t] += m;
#pragma unroll
        for (int e = 0; e < 8; ++e) x[e] -= m;
      };
      fix(0, x0); fix(1, x1); fix(2, x2);
#pragma unroll
      for (int e = 0; e < 8; ++e) EX(e0, x0, e);
      w0 = u32x4{cvtpk(e0[0], e0[1]), cvtpk(e0[2], e0[3]), cvtpk(e0[4], e0[5]), cvtpk(e0[6], e0[7])};
    }
    const bf16x8 p0 = *reinterpret_cast<bf16x8*>(&w0);
    SB();
    lacc[0] = MFMA(ones, p0, lacc[0]); EX(e1, x1, 0); SB();
    PVM(0, 0, p0); EX(e1, x1, 1); SB();
    PVM(0, 1, p0); EX(e1, x1, 2); SB();
    PVM(0, 2, p0); EX(e1, x1, 3); SB();
    PVM(0, 3, p0); EX(e1, x1, 4); SB();
    PVM(0, 4, p0); EX(e1, x1, 5); SB();
    PVM(0, 5, p0); EX(e1, x1, 6); SB();
    PVM(0, 6, p0); EX(e1, x1, 7); SB();
    PVM(0, 7, p0); w1[0] = cvtpk(e1[0], e1[1]); w1[1] = cvtpk(e1[2], e1[3]); SB();
    if (!LAST) {
      dma_block(ldsV, so_w + ((j + 1 + rot) & 7) * 8192, srdV, vvb, vvb ^ 128u, vvb, vvb ^ 128u);
    }
    w1[2] = cvtpk(e1[4], e1[5]); w1[3] = cvtpk(e1[6], e1[7]); EX(e2, x2, 0);
    const bf16x8 p1 = *reinterpret_cast<bf16x8*>(&w1);
    SB();
    lacc[1] = MFMA(ones, p1, lacc[1]); EX(e2, x2, 1); SB();
    PVM(1, 0, p1); EX(e2, x2, 2); SB();
    PVM(1, 1, p1); EX(e2, x2, 3); SB();
    PVM(1, 2, p1); EX(e2, x2, 4); SB();
    PVM(1, 3, p1); EX(e2, x2, 5); SB();
    PVM(1, 4, p1); EX(e2, x2, 6); SB();
    PVM(1, 5, p1); EX(e2, x2, 7); SB();
    PVM(1, 6, p1); w2[0] = cvtpk(e2[0], e2[1]); w2[1] = cvtpk(e2[2], e2[3]); SB();
    PVM(1, 7, p1); w2[2] = cvtpk(e2[4], e2[5]); w2[3] = cvtpk(e2[6], e2[7]); SB();
    const bf16x8 p2 = *reinterpret_cast<bf16x8*>(&w2);
    {
      wait_vm<8>();
#pragma unroll
      for (int u = 0; u < 2; ++u)
#pragma unroll
        for (int s = 0; s < 4; ++s) kf[u][s] = *(const lds_bf16x8*)(uintptr_t)((kab ^ (unsigned)(s << 6)) + u * 4096);
    }
    SB();
    lacc[2] = MFMA(ones, p2, lacc[2]);
#pragma unroll
    for (int n = 0; n < 8; ++n) PVM(2, n, p2);
    SB();
  };
  body(std::true_type{}, 0);
#pragma unroll 1
  for (int j = 1; j < NBLK; ++j) body(std::false_type{}, j);
  STAMP(T2);

  float* st = reinterpret_cast<float*>(smem + LDS_ST);
#pragma unroll
  for (int t = 0; t < 3; ++t)
    if (g == 0) *reinterpret_cast<f32x2*>(st + (wid * SQ + t * 16 + c) * 2) = f32x2{mC[t], lacc[t][0]};
  {
    u32x4* mine = reinterpret_cast<u32x4*>(smem + wid * 16384);
#pragma unroll
    for (int n = 0; n < 8; ++n)
      mine[n * 64 + lane] = u32x4{cvtpk(oacc[n][0][0], oacc[n][0][1]), cvtpk(oacc[n][0][2], oacc[n][0][3]),
                                  cvtpk(oacc[n][1][0], oacc[n][1][1]), cvtpk(oacc[n][1][2], oacc[n][1][3])};
    u32x2* mine2 = reinterpret_cast<u32x2*>(smem + wid * 16384 + 8192);
#pragma unroll
    for (int n = 0; n < 8; ++n)
      mine2[n * 64 + lane] = u32x2{cvtpk(oacc[n][2][0], oacc[n][2][1]), cvtpk(oacc[n][2][2], oacc[n][2][3])};
  }
  STAMP(T3);
  __syncthreads();
  STAMP(T4);
  u32x4 pa[NW]; u32x2 pb[NW];
#pragma unroll
  for (int w = 0; w < NW; ++w) {
    pa[w] = reinterpret_cast<const u32x4*>(smem + w * 16384)[wid * 64 + lane];
    pb[w] = reinterpret_cast<const u32x2*>(smem + w * 16384 + 8192)[wid * 64 + lane];
  }
  __hip_bfloat16* op = o + ((size_t)qhead * SQ + c) * DH + wid * 16 + g * 4;
#pragma unroll
  for (int t = 0; t < 3; ++t) {
    f32x2 s8[NW];
#pragma unroll
    for (int w = 0; w < NW; ++w) s8[w] = *reinterpret_cast<const f32x2*>(st + (w * SQ + t * 16 + c) * 2);
    float M = s8[0][0];
#pragma unroll
    for (int w = 1; w < NW; ++w) M = fmaxf(M, s8[w][0]);
    float L = 0.f;
    f32x4 a = {0.f, 0.f, 0.f, 0.f};
#pragma unroll
    for (int w = 0; w < NW; ++w) {
      const float f = __builtin_amdgcn_exp2f(s8[w][0] - M);
      L = fmaf(s8[w][1], f, L);
      const unsigned lo = t == 0 ? pa[w][0] : t == 1 ? pa[w][2] : pb[w][0];
      const unsigned hi = t == 0 ? pa[w][1] : t == 1 ? pa[w][3] : pb[w][1];
      a[0] = fmaf(__uint_as_float(lo << 16), f, a[0]);
      a[1] = fmaf(__uint_as_float(lo & 0xffff0000u), f, a[1]);
      a[2] = fmaf(__uint_as_float(hi << 16), f, a[2]);
      a[3] = fmaf(__uint_as_float(hi & 0xffff0000u), f, a[3]);
    }
    const float inv = __builtin_amdgcn_rcpf(L);
    u32x2 pk = {cvtpk(a[0] * inv, a[1] * inv), cvtpk(a[2] * inv, a[3] * inv)};
    *reinterpret_cast<u32x2*>(op + t * 16 * DH) = pk;
  }
  STAMP(T5);
  STAMP(T6);
}

extern "C" void kernel_launch(void* const* d_in, const int* in_sizes, int n_in,
                              void* d_out, int out_size, void* d_ws, size_t ws_size,
                              hipStream_t stream) {
  const __hip_bfloat16* q = (const __hip_bfloat16*)d_in[0];
  const __hip_bfloat16* k = (const __hip_bfloat16*)d_in[1];
  const __hip_bfloat16* v = (const __hip_bfloat16*)d_in[2];
  __hip_bfloat16* o = (__hip_bfloat16*)d_out;
  gqa_fwd_kernel<<<dim3(NB_ * HQ), dim3(NW * 64), 0, stream>>>(q, k, v, o
  );
}
```

```cpp
#include <hip/hip_runtime.h>
#include <hip/hip_bf16.h>
#include <stdint.h>
#include <type_traits>

typedef __attribute__((ext_vector_type(8))) short bf16x8;
typedef __attribute__((ext_vector_type(4))) short s16x4;
typedef __attribute__((ext_vector_type(4))) float f32x4;
typedef __attribute__((ext_vector_type(2))) float f32x2;
typedef __attribute__((ext_vector_type(4))) unsigned u32x4;
typedef __attribute__((ext_vector_type(2))) unsigned u32x2;

constexpr int NB_ = 8, HQ = 32, HKV = 4, SQ = 48, SK = 2048, DH = 128;
constexpr int NW = 8;
constexpr int KPW = SK / NW;
constexpr int KB = 32;
constexpr int NBLK = KPW / KB;
constexpr float CSC = 0.088388347648318440f * 1.4426950408889634f;
constexpr float THR2 = 10.0f;

constexpr int LDS_KV = NW * 16384;
constexpr int LDS_Q = LDS_KV;
constexpr int LDS_ST = LDS_Q + SQ * 256;
constexpr int LDS_TOTAL = LDS_ST + NW * SQ * 2 * 4;


typedef __attribute__((ext_vector_type(2))) __bf16 bf16x2;
__device__ __forceinline__ unsigned cvtpk(float lo, float hi) {
  f32x2 v = {lo, hi};
  return __builtin_bit_cast(unsigned, __builtin_convertvector(v, bf16x2));
}
__device__ __forceinline__ float xmax_g(float x) {
  auto a = __builtin_amdgcn_permlane16_swap(__float_as_uint(x), __float_as_uint(x), false, false);
  x = fmaxf(__uint_as_float(a[0]), __uint_as_float(a[1]));
  auto b = __builtin_amdgcn_permlane32_swap(__float_as_uint(x), __float_as_uint(x), false, false);
  return fmaxf(__uint_as_float(b[0]), __uint_as_float(b[1]));
}

__device__ __forceinline__ void dma_block(unsigned lds0, unsigned so0, __amdgpu_buffer_rsrc_t srd,
                                          unsigned v0, unsigned v1, unsigned v2, unsigned v3) {
  asm volatile(
      "s_waitcnt lgkmcnt(0)\n\t"
      "s_mov_b32 m0, %0\n\t"
      "s_nop 0\n\t"
      "buffer_load_dwordx4 %4, %3, %1 offen offset:0 lds\n\t"
      "buffer_load_dwordx4 %5, %3, %1 offen offset:1024 lds\n\t"
      "buffer_load_dwordx4 %6, %3, %1 offen offset:2048 lds\n\t"
      "buffer_load_dwordx4 %7, %3, %1 offen offset:3072 lds\n\t"
      "s_add_u32 m0, %0, 4096\n\t"
      "s_nop 0\n\t"
      "buffer_load_dwordx4 %4, %3, %2 offen offset:0 lds\n\t"
      "buffer_load_dwordx4 %5, %3, %2 offen offset:1024 lds\n\t"
      "buffer_load_dwordx4 %6, %3, %2 offen offset:2048 lds\n\t"
      "buffer_load_dwordx4 %7, %3, %2 offen offset:3072 lds"
      :: "s"(lds0), "s"(so0), "s"(so0 + 4096u), "s"(srd), "v"(v0), "v"(v1), "v"(v2), "v"(v3)
      : "memory", "scc");
}
__device__ __forceinline__ void dma_piece(unsigned lds0, unsigned so0, __amdgpu_buffer_rsrc_t srd, unsigned v0) {
  asm volatile("s_mov_b32 m0, %0\n\ts_nop 0\n\tbuffer_load_dwordx4 %3, %2, %1 offen lds"
               :: "s"(lds0), "s"(so0), "s"(srd), "v"(v0) : "memory");
}
template <int N> __device__ __forceinline__ void wait_vm() { asm volatile("s_waitcnt vmcnt(%0)" :: "i"(N) : "memory"); }

typedef __attribute__((address_space(3))) s16x4 lds_s16x4;
typedef __attribute__((address_space(3))) bf16x8 lds_bf16x8;

__global__ __launch_bounds__(NW * 64, 2)
void gqa_fwd_kernel(const __hip_bfloat16* __restrict__ q, const __hip_bfloat16* __restrict__ k,
                    const __hip_bfloat16* __restrict__ v, __hip_bfloat16* __restrict__ o
                    ) {
  __shared__ __attribute__((aligned(1024))) char smem[LDS_TOTAL];
  const int tid = threadIdx.x, lane = tid & 63;
  const int wid = __builtin_amdgcn_readfirstlane(tid >> 6);
  const int c = lane & 15, g = lane >> 4;
  const int bid = blockIdx.x, xcd = bid & 7, idx = bid >> 3;
  const int grp = xcd * 4 + (idx >> 3), hh = idx & 7;
  const int qhead = grp * 8 + hh;
  const char* Kg = (const char*)k + (size_t)grp * SK * DH * 2;
  const char* Vg = (const char*)v + (size_t)grp * SK * DH * 2;
  const char* Qg = (const char*)q + (size_t)qhead * SQ * DH * 2;
  const __amdgpu_buffer_rsrc_t srdK = __builtin_amdgcn_make_buffer_rsrc((void*)Kg, (short)0, SK * DH * 2, 0x00020000);
  const __amdgpu_buffer_rsrc_t srdV = __builtin_amdgcn_make_buffer_rsrc((void*)Vg, (short)0, SK * DH * 2, 0x00020000);
  const __amdgpu_buffer_rsrc_t srdQ = __builtin_amdgcn_make_buffer_rsrc((void*)Qg, (short)0, SQ * DH * 2, 0x00020000);

  const unsigned lds_base = (unsigned)(uintptr_t)smem;
  const unsigned ldsK = lds_base + wid * 16384, ldsV = ldsK + 8192;
  const unsigned rr = lane >> 4, pos = lane & 15;
  const unsigned kv0 = rr * 256 + ((pos ^ (0 + rr)) << 4), kv1 = rr * 256 + ((pos ^ (4 + rr)) << 4);
  const unsigned kv2 = rr * 256 + ((pos ^ (8 + rr)) << 4), kv3 = rr * 256 + ((pos ^ (12 + rr)) << 4);
  const unsigned vv0 = rr * 256 + ((pos ^ (2 * rr)) << 4), vv1 = rr * 256 + ((pos ^ (2 * (4 + rr))) << 4);
  const unsigned so_w = (unsigned)wid * KPW * 256;

  if (wid == 7) {
    const char* pc = (const char*)(__builtin_amdgcn_s_getpc() & ~(uintptr_t)127);
    const char* p0 = pc + lane * 128;
    asm volatile("s_mov_b32 m0, %0\n\ts_nop 0\n\tglobal_load_lds_dword %1, off" :: "s"(lds_base + LDS_ST), "v"(p0) : "memory");
  }
  {
    const unsigned qv = rr * 256 + ((pos ^ (4 * (wid & 3) + rr)) << 4);
    dma_piece(lds_base + LDS_Q + wid * 1024, wid * 1024, srdQ, qv);
    if (wid < 4) dma_piece(lds_base + LDS_Q + (wid + 8) * 1024, (wid + 8) * 1024, srdQ, qv);
  }
  __builtin_amdgcn_s_barrier();
  dma_block(ldsK, so_w, srdK, kv0, kv1, kv2, kv3);
  __builtin_amdgcn_s_barrier();
  dma_block(ldsV, so_w, srdV, vv0, vv1, vv0, vv1);
  wait_vm<16>();

  unsigned ka[4];
#pragma unroll
  for (int s = 0; s < 4; ++s) ka[s] = (unsigned)(c * 256 + (((4 * s + g) ^ c) << 4));
  const char* kr = smem + wid * 16384;
  const int qq = (lane & 15) >> 2, pp = lane & 3;
  const int vkey = 4 * g + qq, k7 = vkey & 7;

  __builtin_amdgcn_s_barrier();
  asm volatile("" ::: "memory");
  bf16x8 qf[3][4];
#pragma unroll
  for (int t = 0; t < 3; ++t)
#pragma unroll
    for (int s = 0; s < 4; ++s) qf[t][s] = *reinterpret_cast<const bf16x8*>(smem + LDS_Q + t * 4096 + ka[s]);

  f32x4 oacc[8][3], lacc[3];
#pragma unroll
  for (int t = 0; t < 3; ++t) {
    lacc[t] = f32x4{0.f, 0.f, 0.f, 0.f};
#pragma unroll
    for (int n = 0; n < 8; ++n) oacc[n][t] = f32x4{0.f, 0.f, 0.f, 0.f};
  }
  float mC[3] = {0.f, 0.f, 0.f};
  const bf16x8 ones = {0x3F80, 0x3F80, 0x3F80, 0x3F80, 0x3F80, 0x3F80, 0x3F80, 0x3F80};
  wait_vm<8>();
  bf16x8 kf[2][4];
#pragma unroll
  for (int u = 0; u < 2; ++u)
#pragma unroll
    for (int s = 0; s < 4; ++s) kf[u][s] = *reinterpret_cast<const bf16x8*>(kr + u * 4096 + ka[s]);

  unsigned kvb = kv0, vvb = vv0;
  unsigned kab = ldsK + ka[0];
  unsigned vab = ldsV + vkey * 256 + (k7 << 5) + (pp >> 1) * 16 + (pp & 1) * 8;
#define SB() __builtin_amdgcn_sched_barrier(0)
#define MFMA(a, b, c) __builtin_amdgcn_mfma_f32_16x16x32_bf16(a, b, c, 0, 0, 0)
#define QKM(S, t, i) S[(i) & 1] = MFMA(kf[(i) & 1][(i) >> 1], qf[t][(i) >> 1], S[(i) & 1])
#define XF(X, S, t, e) X[e] = fmaf(S[(e) >> 2][(e) & 3], CSC, -mC[t])
#define EX(P, X, e) P[e] = __builtin_amdgcn_exp2f(X[e])
#define PVM(t, n, pf) oacc[n][t] = MFMA(vf[n], pf, oacc[n][t])
  auto body = [&](auto FIRST_, int j) {
    constexpr bool FIRST = decltype(FIRST_)::value;
    const bool LAST = j == NBLK - 1;
    f32x4 s0[2] = {f32x4{0.f, 0.f, 0.f, 0.f}, f32x4{0.f, 0.f, 0.f, 0.f}};
    f32x4 s1[2] = {f32x4{0.f, 0.f, 0.f, 0.f}, f32x4{0.f, 0.f, 0.f, 0.f}};
    f32x4 s2[2] = {f32x4{0.f, 0.f, 0.f, 0.f}, f32x4{0.f, 0.f, 0.f, 0.f}};
    float x0[8], x1[8], x2[8], e0[8], e1[8], e2[8];
    u32x4 w0, w1, w2;
    float mx;

    asm volatile("" : "+v"(kvb), "+v"(vvb), "+v"(kab));
    QKM(s0, 0, 0); QKM(s0, 0, 1); SB();
    if (!LAST) dma_block(ldsK, so_w + (j + 1) * 8192, srdK, kvb, kvb ^ 64u, kvb ^ 128u, kvb ^ 192u);
    SB();
    QKM(s0, 0, 2); QKM(s0, 0, 3); QKM(s0, 0, 4); QKM(s0, 0, 5); QKM(s0, 0, 6); QKM(s0, 0, 7); SB();
    QKM(s1, 1, 0); SB();
    QKM(s1, 1, 1); SB();
    QKM(s1, 1, 2); XF(x0, s0, 0, 0); XF(x0, s0, 0, 1); SB();
    QKM(s1, 1, 3); XF(x0, s0, 0, 2); XF(x0, s0, 0, 3); SB();
    QKM(s1, 1, 4); XF(x0, s0, 0, 4); XF(x0, s0, 0, 5); SB();
    QKM(s1, 1, 5); XF(x0, s0, 0, 6); XF(x0, s0, 0, 7); SB();
    QKM(s1, 1, 6); EX(e0, x0, 0); SB();
    QKM(s1, 1, 7); EX(e0, x0, 1); SB();
    QKM(s2, 2, 0); EX(e0, x0, 2); SB();
    QKM(s2, 2, 1); EX(e0, x0, 3); SB();
    QKM(s2, 2, 2); EX(e0, x0, 4); SB();
    QKM(s2, 2, 3); EX(e0, x0, 5); SB();
    QKM(s2, 2, 4); EX(e0, x0, 6); SB();
    QKM(s2, 2, 5); EX(e0, x0, 7); SB();
    QKM(s2, 2, 6); XF(x1, s1, 1, 0); XF(x1, s1, 1, 1); SB();
    QKM(s2, 2, 7); XF(x1, s1, 1, 2); XF(x1, s1, 1, 3); SB();
    if (!LAST) wait_vm<8>(); else wait_vm<0>();
    bf16x8 vf[8];
#pragma unroll
    for (int n = 0; n < 8; ++n) {
      const unsigned a = vab ^ (unsigned)(n << 5);
      const s16x4 lo = __builtin_amdgcn_ds_read_tr16_b64_v4i16((lds_s16x4*)(uintptr_t)(a));
      const s16x4 hi = __builtin_amdgcn_ds_read_tr16_b64_v4i16((lds_s16x4*)(uintptr_t)(a + 4096));
      vf[n] = bf16x8{lo[0], lo[1], lo[2], lo[3], hi[0], hi[1], hi[2], hi[3]};
    }
    SB();
    XF(x1, s1, 1, 4); XF(x1, s1, 1, 5); XF(x1, s1, 1, 6); XF(x1, s1, 1, 7);
    w0 = u32x4{cvtpk(e0[0], e0[1]), cvtpk(e0[2], e0[3]), cvtpk(e0[4], e0[5]), cvtpk(e0[6], e0[7])};
#pragma unroll
    for (int e = 0; e < 8; ++e) XF(x2, s2, 2, e);
    mx = fmaxf(fmaxf(x0[0], x0[1]), x0[2]);
    mx = fmaxf(fmaxf(mx, x0[3]), x0[4]);
    mx = fmaxf(fmaxf(mx, x0[5]), x0[6]);
    mx = fmaxf(fmaxf(mx, x0[7]), x1[0]);
#pragma unroll
    for (int e = 1; e < 7; e += 2) mx = fmaxf(fmaxf(mx, x1[e]), x1[e + 1]);
    mx = fmaxf(fmaxf(mx, x1[7]), x2[0]);
#pragma unroll
    for (int e = 1; e < 7; e += 2) mx = fmaxf(fmaxf(mx, x2[e]), x2[e + 1]);
    mx = fmaxf(mx, x2[7]);
    if (FIRST || __any(!(mx <= THR2))) {
      auto fix = [&](int t, float (&x)[8]) {
        float m = x[0];
#pragma unroll
        for (int e = 1; e < 8; ++e) m = fmaxf(m, x[e]);
        m = xmax_g(m);
        if constexpr (!FIRST) {
          m = fmaxf(m, 0.f);
          const float al = __builtin_amdgcn_exp2f(-m);
          lacc[t] *= al;
#pragma unroll
          for (int n = 0; n < 8; ++n) oacc[n][t] *= al;
        }
        mC[t] += m;
#pragma unroll
        for (int e = 0; e < 8; ++e) x[e] -= m;
      };
      fix(0, x0); fix(1, x1); fix(2, x2);
#pragma unroll
      for (int e = 0; e < 8; ++e) EX(e0, x0, e);
      w0 = u32x4{cvtpk(e0[0], e0[1]), cvtpk(e0[2], e0[3]), cvtpk(e0[4], e0[5]), cvtpk(e0[6], e0[7])};
    }
    const bf16x8 p0 = *reinterpret_cast<bf16x8*>(&w0);
    SB();
    lacc[0] = MFMA(ones, p0, lacc[0]); EX(e1, x1, 0); SB();
    PVM(0, 0, p0); EX(e1, x1, 1); SB();
    PVM(0, 1, p0); EX(e1, x1, 2); SB();
    PVM(0, 2, p0); EX(e1, x1, 3); SB();
    PVM(0, 3, p0); EX(e1, x1, 4); SB();
    PVM(0, 4, p0); EX(e1, x1, 5); SB();
    PVM(0, 5, p0); EX(e1, x1, 6); SB();
    PVM(0, 6, p0); EX(e1, x1, 7); SB();
    PVM(0, 7, p0); w1[0] = cvtpk(e1[0], e1[1]); w1[1] = cvtpk(e1[2], e1[3]); SB();
    if (!LAST) {
      dma_block(ldsV, so_w + (j + 1) * 8192, srdV, vvb, vvb ^ 128u, vvb, vvb ^ 128u);
    }
    w1[2] = cvtpk(e1[4], e1[5]); w1[3] = cvtpk(e1[6], e1[7]); EX(e2, x2, 0);
    const bf16x8 p1 = *reinterpret_cast<bf16x8*>(&w1);
    SB();
    lacc[1] = MFMA(ones, p1, lacc[1]); EX(e2, x2, 1); SB();
    PVM(1, 0, p1); EX(e2, x2, 2); SB();
    PVM(1, 1, p1); EX(e2, x2, 3); SB();
    PVM(1, 2, p1); EX(e2, x2, 4); SB();
    PVM(1, 3, p1); EX(e2, x2, 5); SB();
    PVM(1, 4, p1); EX(e2, x2, 6); SB();
    PVM(1, 5, p1); EX(e2, x2, 7); SB();
    PVM(1, 6, p1); w2[0] = cvtpk(e2[0], e2[1]); w2[1] = cvtpk(e2[2], e2[3]); SB();
    PVM(1, 7, p1); w2[2] = cvtpk(e2[4], e2[5]); w2[3] = cvtpk(e2[6], e2[7]); SB();
    const bf16x8 p2 = *reinterpret_cast<bf16x8*>(&w2);
    {
      wait_vm<8>();
#pragma unroll
      for (int u = 0; u < 2; ++u)
#pragma unroll
        for (int s = 0; s < 4; ++s) kf[u][s] = *(const lds_bf16x8*)(uintptr_t)((kab ^ (unsigned)(s << 6)) + u * 4096);
    }
    SB();
    lacc[2] = MFMA(ones, p2, lacc[2]);
#pragma unroll
    for (int n = 0; n < 8; ++n) PVM(2, n, p2);
    SB();
  };
  body(std::true_type{}, 0);
#pragma unroll 1
  for (int j = 1; j < NBLK; ++j) body(std::false_type{}, j);

  float* st = reinterpret_cast<float*>(smem + LDS_ST);
#pragma unroll
  for (int t = 0; t < 3; ++t)
    if (g == 0) *reinterpret_cast<f32x2*>(st + (wid * SQ + t * 16 + c) * 2) = f32x2{mC[t], lacc[t][0]};
  {
    u32x4* mine = reinterpret_cast<u32x4*>(smem + wid * 16384);
#pragma unroll
    for (int n = 0; n < 8; ++n)
      mine[n * 64 + lane] = u32x4{cvtpk(oacc[n][0][0], oacc[n][0][1]), cvtpk(oacc[n][0][2], oacc[n][0][3]),
                                  cvtpk(oacc[n][1][0], oacc[n][1][1]), cvtpk(oacc[n][1][2], oacc[n][1][3])};
    u32x2* mine2 = reinterpret_cast<u32x2*>(smem + wid * 16384 + 8192);
#pragma unroll
    for (int n = 0; n < 8; ++n)
      mine2[n * 64 + lane] = u32x2{cvtpk(oacc[n][2][0], oacc[n][2][1]), cvtpk(oacc[n][2][2], oacc[n][2][3])};
  }
  __syncthreads();
  u32x4 pa[NW]; u32x2 pb[NW];
#pragma unroll
  for (int w = 0; w < NW; ++w) {
    pa[w] = reinterpret_cast<const u32x4*>(smem + w * 16384)[wid * 64 + lane];
    pb[w] = reinterpret_cast<const u32x2*>(smem + w * 16384 + 8192)[wid * 64 + lane];
  }
  __hip_bfloat16* op = o + ((size_t)qhead * SQ + c) * DH + wid * 16 + g * 4;
#pragma unroll
  for (int t = 0; t < 3; ++t) {
    f32x2 s8[NW];
#pragma unroll
    for (int w = 0; w < NW; ++w) s8[w] = *reinterpret_cast<const f32x2*>(st + (w * SQ + t * 16 + c) * 2);
    float M = s8[0][0];
#pragma unroll
    for (int w = 1; w < NW; ++w) M = fmaxf(M, s8[w][0]);
    float L = 0.f;
    f32x4 a = {0.f, 0.f, 0.f, 0.f};
#pragma unroll
    for (int w = 0; w < NW; ++w) {
      const float f = __builtin_amdgcn_exp2f(s8[w][0] - M);
      L = fmaf(s8[w][1], f, L);
      const unsigned lo = t == 0 ? pa[w][0] : t == 1 ? pa[w][2] : pb[w][0];
      const unsigned hi = t == 0 ? pa[w][1] : t == 1 ? pa[w][3] : pb[w][1];
      a[0] = fmaf(__uint_as_float(lo << 16), f, a[0]);
      a[1] = fmaf(__uint_as_float(lo & 0xffff0000u), f, a[1]);
      a[2] = fmaf(__uint_as_float(hi << 16), f, a[2]);
      a[3] = fmaf(__uint_as_float(hi & 0xffff0000u), f, a[3]);
    }
    const float inv = __builtin_amdgcn_rcpf(L);
    u32x2 pk = {cvtpk(a[0] * inv, a[1] * inv), cvtpk(a[2] * inv, a[3] * inv)};
    *reinterpret_cast<u32x2*>(op + t * 16 * DH) = pk;
  }
}

extern "C" void kernel_launch(void* const* d_in, const int* in_sizes, int n_in,
                              void* d_out, int out_size, void* d_ws, size_t ws_size,
                              hipStream_t stream) {
  const __hip_bfloat16* q = (const __hip_bfloat16*)d_in[0];
  const __hip_bfloat16* k = (const __hip_bfloat16*)d_in[1];
  const __hip_bfloat16* v = (const __hip_bfloat16*)d_in[2];
  __hip_bfloat16* o = (__hip_bfloat16*)d_out;
  gqa_fwd_kernel<<<dim3(NB_ * HQ), dim3(NW * 64), 0, stream>>>(q, k, v, o
  );
}
```

```cpp
#include <hip/hip_runtime.h>
#include <hip/hip_bf16.h>
#include <stdint.h>
#include <type_traits>

typedef __attribute__((ext_vector_type(8))) short bf16x8;
typedef __attribute__((ext_vector_type(4))) short s16x4;
typedef __attribute__((ext_vector_type(4))) float f32x4;
typedef __attribute__((ext_vector_type(2))) float f32x2;
typedef __attribute__((ext_vector_type(4))) unsigned u32x4;
typedef __attribute__((ext_vector_type(2))) unsigned u32x2;

constexpr int NB_ = 8, HQ = 32, HKV = 4, SQ = 48, SK = 2048, DH = 128;
constexpr int NW = 8;
constexpr int KPW = SK / NW;
constexpr int KB = 32;
constexpr int NBLK = KPW / KB;
constexpr float CSC = 0.088388347648318440f * 1.4426950408889634f;
constexpr float THR2 = 10.0f;

constexpr int LDS_KV = NW * 16384;
constexpr int LDS_Q = LDS_KV;
constexpr int LDS_ST = LDS_Q + SQ * 256;
constexpr int LDS_TOTAL = LDS_ST + NW * SQ * 2 * 4;


typedef __attribute__((ext_vector_type(2))) __bf16 bf16x2;
__device__ __forceinline__ unsigned cvtpk(float lo, float hi) {
  f32x2 v = {lo, hi};
  return __builtin_bit_cast(unsigned, __builtin_convertvector(v, bf16x2));
}
__device__ __forceinline__ float xmax_g(float x) {
  auto a = __builtin_amdgcn_permlane16_swap(__float_as_uint(x), __float_as_uint(x), false, false);
  x = fmaxf(__uint_as_float(a[0]), __uint_as_float(a[1]));
  auto b = __builtin_amdgcn_permlane32_swap(__float_as_uint(x), __float_as_uint(x), false, false);
  return fmaxf(__uint_as_float(b[0]), __uint_as_float(b[1]));
}

__device__ __forceinline__ void dma_block(unsigned lds0, unsigned so0, __amdgpu_buffer_rsrc_t srd,
                                          unsigned v0, unsigned v1, unsigned v2, unsigned v3) {
  asm volatile(
      "s_waitcnt lgkmcnt(0)\n\t"
      "s_mov_b32 m0, %0\n\t"
      "s_nop 0\n\t"
      "buffer_load_dwordx4 %4, %3, %1 offen offset:0 lds\n\t"
      "buffer_load_dwordx4 %5, %3, %1 offen offset:1024 lds\n\t"
      "buffer_load_dwordx4 %6, %3, %1 offen offset:2048 lds\n\t"
      "buffer_load_dwordx4 %7, %3, %1 offen offset:3072 lds\n\t"
      "s_add_u32 m0, %0, 4096\n\t"
      "s_nop 0\n\t"
      "buffer_load_dwordx4 %4, %3, %2 offen offset:0 lds\n\t"
      "buffer_load_dwordx4 %5, %3, %2 offen offset:1024 lds\n\t"
      "buffer_load_dwordx4 %6, %3, %2 offen offset:2048 lds\n\t"
      "buffer_load_dwordx4 %7, %3, %2 offen offset:3072 lds"
      :: "s"(lds0), "s"(so0), "s"(so0 + 4096u), "s"(srd), "v"(v0), "v"(v1), "v"(v2), "v"(v3)
      : "memory", "scc");
}
__device__ __forceinline__ void dma_piece(unsigned lds0, unsigned so0, __amdgpu_buffer_rsrc_t srd, unsigned v0) {
  asm volatile("s_mov_b32 m0, %0\n\ts_nop 0\n\tbuffer_load_dwordx4 %3, %2, %1 offen lds"
               :: "s"(lds0), "s"(so0), "s"(srd), "v"(v0) : "memory");
}
template <int N> __device__ __forceinline__ void wait_vm() { asm volatile("s_waitcnt vmcnt(%0)" :: "i"(N) : "memory"); }

typedef __attribute__((address_space(3))) s16x4 lds_s16x4;
typedef __attribute__((address_space(3))) bf16x8 lds_bf16x8;

__global__ __launch_bounds__(NW * 64, 2)
void gqa_fwd_kernel(const __hip_bfloat16* __restrict__ q, const __hip_bfloat16* __restrict__ k,
                    const __hip_bfloat16* __restrict__ v, __hip_bfloat16* __restrict__ o
                    ) {
  __shared__ __attribute__((aligned(1024))) char smem[LDS_TOTAL];
  const int tid = threadIdx.x, lane = tid & 63;
  const int wid = __builtin_amdgcn_readfirstlane(tid >> 6);
  const int c = lane & 15, g = lane >> 4;
  const int bid = blockIdx.x, xcd = bid & 7, idx = bid >> 3;
  const int grp = xcd * 4 + (idx >> 3), hh = idx & 7;
  const int qhead = grp * 8 + hh;
  const char* Kg = (const char*)k + (size_t)grp * SK * DH * 2;
  const char* Vg = (const char*)v + (size_t)grp * SK * DH * 2;
  const char* Qg = (const char*)q + (size_t)qhead * SQ * DH * 2;
  const __amdgpu_buffer_rsrc_t srdK = __builtin_amdgcn_make_buffer_rsrc((void*)Kg, (short)0, SK * DH * 2, 0x00020000);
  const __amdgpu_buffer_rsrc_t srdV = __builtin_amdgcn_make_buffer_rsrc((void*)Vg, (short)0, SK * DH * 2, 0x00020000);
  const __amdgpu_buffer_rsrc_t srdQ = __builtin_amdgcn_make_buffer_rsrc((void*)Qg, (short)0, SQ * DH * 2, 0x00020000);

  const unsigned lds_base = (unsigned)(uintptr_t)smem;
  const unsigned ldsK = lds_base + wid * 16384, ldsV = ldsK + 8192;
  const unsigned rr = lane >> 4, pos = lane & 15;
  const unsigned kv0 = rr * 256 + ((pos ^ (0 + rr)) << 4), kv1 = rr * 256 + ((pos ^ (4 + rr)) << 4);
  const unsigned kv2 = rr * 256 + ((pos ^ (8 + rr)) << 4), kv3 = rr * 256 + ((pos ^ (12 + rr)) << 4);
  const unsigned vv0 = rr * 256 + ((pos ^ (2 * rr)) << 4), vv1 = rr * 256 + ((pos ^ (2 * (4 + rr))) << 4);
  const unsigned so_w = (unsigned)wid * KPW * 256;

  {
    const unsigned qv = rr * 256 + ((pos ^ (4 * (wid & 3) + rr)) << 4);
    dma_piece(lds_base + LDS_Q + wid * 1024, wid * 1024, srdQ, qv);
    if (wid < 4) dma_piece(lds_base + LDS_Q + (wid + 8) * 1024, (wid + 8) * 1024, srdQ, qv);
  }
  __builtin_amdgcn_s_barrier();
  dma_block(ldsK, so_w, srdK, kv0, kv1, kv2, kv3);
  __builtin_amdgcn_s_barrier();
  dma_block(ldsV, so_w, srdV, vv0, vv1, vv0, vv1);
  wait_vm<16>();

  unsigned ka[4];
#pragma unroll
  for (int s = 0; s < 4; ++s) ka[s] = (unsigned)(c * 256 + (((4 * s + g) ^ c) << 4));
  const char* kr = smem + wid * 16384;
  const int qq = (lane & 15) >> 2, pp = lane & 3;
  const int vkey = 4 * g + qq, k7 = vkey & 7;

  __builtin_amdgcn_s_barrier();
  asm volatile("" ::: "memory");
  bf16x8 qf[3][4];
#pragma unroll
  for (int t = 0; t < 3; ++t)
#pragma unroll
    for (int s = 0; s < 4; ++s) qf[t][s] = *reinterpret_cast<const bf16x8*>(smem + LDS_Q + t * 4096 + ka[s]);

  f32x4 oacc[8][3], lacc[3];
#pragma unroll
  for (int t = 0; t < 3; ++t) {
    lacc[t] = f32x4{0.f, 0.f, 0.f, 0.f};
#pragma unroll
    for (int n = 0; n < 8; ++n) oacc[n][t] = f32x4{0.f, 0.f, 0.f, 0.f};
  }
  float mC[3] = {0.f, 0.f, 0.f};
  const bf16x8 ones = {0x3F80, 0x3F80, 0x3F80, 0x3F80, 0x3F80, 0x3F80, 0x3F80, 0x3F80};
  wait_vm<8>();
  bf16x8 kf[2][4];
#pragma unroll
  for (int u = 0; u < 2; ++u)
#pragma unroll
    for (int s = 0; s < 4; ++s) kf[u][s] = *reinterpret_cast<const bf16x8*>(kr + u * 4096 + ka[s]);

  unsigned kvb = kv0, vvb = vv0;
  unsigned kab = ldsK + ka[0];
  unsigned vab = ldsV + vkey * 256 + (k7 << 5) + (pp >> 1) * 16 + (pp & 1) * 8;
#define SB() __builtin_amdgcn_sched_barrier(0)
#define MFMA(a, b, c) __builtin_amdgcn_mfma_f32_16x16x32_bf16(a, b, c, 0, 0, 0)
#define QKM(S, t, i) S[(i) & 1] = MFMA(kf[(i) & 1][(i) >> 1], qf[t][(i) >> 1], S[(i) & 1])
#define XF(X, S, t, e) X[e] = fmaf(S[(e) >> 2][(e) & 3], CSC, -mC[t])
#define EX(P, X, e) P[e] = __builtin_amdgcn_exp2f(X[e])
#define PVM(t, n, pf) oacc[n][t] = MFMA(vf[n], pf, oacc[n][t])
  auto body = [&](auto FIRST_, int j) {
    constexpr bool FIRST = decltype(FIRST_)::value;
    const bool LAST = j == NBLK - 1;
    f32x4 s0[2] = {f32x4{0.f, 0.f, 0.f, 0.f}, f32x4{0.f, 0.f, 0.f, 0.f}};
    f32x4 s1[2] = {f32x4{0.f, 0.f, 0.f, 0.f}, f32x4{0.f, 0.f, 0.f, 0.f}};
    f32x4 s2[2] = {f32x4{0.f, 0.f, 0.f, 0.f}, f32x4{0.f, 0.f, 0.f, 0.f}};
    float x0[8], x1[8], x2[8], e0[8], e1[8], e2[8];
    u32x4 w0, w1, w2;
    float mx;

    asm volatile("" : "+v"(kvb), "+v"(vvb), "+v"(kab));
    QKM(s0, 0, 0); QKM(s0, 0, 1); SB();
    if (!LAST) dma_block(ldsK, so_w + (j + 1) * 8192, srdK, kvb, kvb ^ 64u, kvb ^ 128u, kvb ^ 192u);
    SB();
    QKM(s0, 0, 2); QKM(s0, 0, 3); QKM(s0, 0, 4); QKM(s0, 0, 5); QKM(s0, 0, 6); QKM(s0, 0, 7); SB();
    QKM(s1, 1, 0); SB();
    QKM(s1, 1, 1); SB();
    QKM(s1, 1, 2); XF(x0, s0, 0, 0); XF(x0, s0, 0, 1); SB();
    QKM(s1, 1, 3); XF(x0, s0, 0, 2); XF(x0, s0, 0, 3); SB();
    QKM(s1, 1, 4); XF(x0, s0, 0, 4); XF(x0, s0, 0, 5); SB();
    QKM(s1, 1, 5); XF(x0, s0, 0, 6); XF(x0, s0, 0, 7); SB();
    QKM(s1, 1, 6); EX(e0, x0, 0); SB();
    QKM(s1, 1, 7); EX(e0, x0, 1); SB();
    QKM(s2, 2, 0); EX(e0, x0, 2); SB();
    QKM(s2, 2, 1); EX(e0, x0, 3); SB();
    QKM(s2, 2, 2); EX(e0, x0, 4); SB();
    QKM(s2, 2, 3); EX(e0, x0, 5); SB();
    QKM(s2, 2, 4); EX(e0, x0, 6); SB();
    QKM(s2, 2, 5); EX(e0, x0, 7); SB();
    QKM(s2, 2, 6); XF(x1, s1, 1, 0); XF(x1, s1, 1, 1); SB();
    QKM(s2, 2, 7); XF(x1, s1, 1, 2); XF(x1, s1, 1, 3); SB();
    if (!LAST) wait_vm<8>(); else wait_vm<0>();
    bf16x8 vf[8];
#pragma unroll
    for (int n = 0; n < 8; ++n) {
      const unsigned a = vab ^ (unsigned)(n << 5);
      const s16x4 lo = __builtin_amdgcn_ds_read_tr16_b64_v4i16((lds_s16x4*)(uintptr_t)(a));
      const s16x4 hi = __builtin_amdgcn_ds_read_tr16_b64_v4i16((lds_s16x4*)(uintptr_t)(a + 4096));
      vf[n] = bf16x8{lo[0], lo[1], lo[2], lo[3], hi[0], hi[1], hi[2], hi[3]};
    }
    SB();
    XF(x1, s1, 1, 4); XF(x1, s1, 1, 5); XF(x1, s1, 1, 6); XF(x1, s1, 1, 7);
    w0 = u32x4{cvtpk(e0[0], e0[1]), cvtpk(e0[2], e0[3]), cvtpk(e0[4], e0[5]), cvtpk(e0[6], e0[7])};
#pragma unroll
    for (int e = 0; e < 8; ++e) XF(x2, s2, 2, e);
    mx = fmaxf(fmaxf(x0[0], x0[1]), x0[2]);
    mx = fmaxf(fmaxf(mx, x0[3]), x0[4]);
    mx = fmaxf(fmaxf(mx, x0[5]), x0[6]);
    mx = fmaxf(fmaxf(mx, x0[7]), x1[0]);
#pragma unroll
    for (int e = 1; e < 7; e += 2) mx = fmaxf(fmaxf(mx, x1[e]), x1[e + 1]);
    mx = fmaxf(fmaxf(mx, x1[7]), x2[0]);
#pragma unroll
    for (int e = 1; e < 7; e += 2) mx = fmaxf(fmaxf(mx, x2[e]), x2[e + 1]);
    mx = fmaxf(mx, x2[7]);
    if (FIRST || __any(!(mx <= THR2))) {
      auto fix = [&](int t, float (&x)[8]) {
        float m = x[0];
#pragma unroll
        for (int e = 1; e < 8; ++e) m = fmaxf(m, x[e]);
        m = xmax_g(m);
        if constexpr (!FIRST) {
          m = fmaxf(m, 0.f);
          const float al = __builtin_amdgcn_exp2f(-m);
          lacc[t] *= al;
#pragma unroll
          for (int n = 0; n < 8; ++n) oacc[n][t] *= al;
        }
        mC[t] += m;
#pragma unroll
        for (int e = 0; e < 8; ++e) x[e] -= m;
      };
      fix(0, x0); fix(1, x1); fix(2, x2);
#pragma unroll
      for (int e = 0; e < 8; ++e) EX(e0, x0, e);
      w0 = u32x4{cvtpk(e0[0], e0[1]), cvtpk(e0[2], e0[3]), cvtpk(e0[4], e0[5]), cvtpk(e0[6], e0[7])};
    }
    const bf16x8 p0 = *reinterpret_cast<bf16x8*>(&w0);
    SB();
    lacc[0] = MFMA(ones, p0, lacc[0]); EX(e1, x1, 0); SB();
    PVM(0, 0, p0); EX(e1, x1, 1); SB();
    PVM(0, 1, p0); EX(e1, x1, 2); SB();
    PVM(0, 2, p0); EX(e1, x1, 3); SB();
    PVM(0, 3, p0); EX(e1, x1, 4); SB();
    PVM(0, 4, p0); EX(e1, x1, 5); SB();
    PVM(0, 5, p0); EX(e1, x1, 6); SB();
    PVM(0, 6, p0); EX(e1, x1, 7); SB();
    PVM(0, 7, p0); w1[0] = cvtpk(e1[0], e1[1]); w1[1] = cvtpk(e1[2], e1[3]); SB();
    if (!LAST) {
      dma_block(ldsV, so_w + (j + 1) * 8192, srdV, vvb, vvb ^ 128u, vvb, vvb ^ 128u);
    }
    w1[2] = cvtpk(e1[4], e1[5]); w1[3] = cvtpk(e1[6], e1[7]); EX(e2, x2, 0);
    const bf16x8 p1 = *reinterpret_cast<bf16x8*>(&w1);
    SB();
    lacc[1] = MFMA(ones, p1, lacc[1]); EX(e2, x2, 1); SB();
    PVM(1, 0, p1); EX(e2, x2, 2); SB();
    PVM(1, 1, p1); EX(e2, x2, 3); SB();
    PVM(1, 2, p1); EX(e2, x2, 4); SB();
    PVM(1, 3, p1); EX(e2, x2, 5); SB();
    PVM(1, 4, p1); EX(e2, x2, 6); SB();
    PVM(1, 5, p1); EX(e2, x2, 7); SB();
    PVM(1, 6, p1); w2[0] = cvtpk(e2[0], e2[1]); w2[1] = cvtpk(e2[2], e2[3]); SB();
    PVM(1, 7, p1); w2[2] = cvtpk(e2[4], e2[5]); w2[3] = cvtpk(e2[6], e2[7]); SB();
    const bf16x8 p2 = *reinterpret_cast<bf16x8*>(&w2);
    {
      wait_vm<8>();
#pragma unroll
      for (int u = 0; u < 2; ++u)
#pragma unroll
        for (int s = 0; s < 4; ++s) kf[u][s] = *(const lds_bf16x8*)(uintptr_t)((kab ^ (unsigned)(s << 6)) + u * 4096);
    }
    SB();
    lacc[2] = MFMA(ones, p2, lacc[2]);
#pragma unroll
    for (int n = 0; n < 8; ++n) PVM(2, n, p2);
    SB();
  };
  body(std::true_type{}, 0);
#pragma unroll 1
  for (int j = 1; j < NBLK; ++j) body(std::false_type{}, j);

  float* st = reinterpret_cast<float*>(smem + LDS_ST);
#pragma unroll
  for (int t = 0; t < 3; ++t)
    if (g == 0) *reinterpret_cast<f32x2*>(st + (wid * SQ + t * 16 + c) * 2) = f32x2{mC[t], lacc[t][0]};
  {
    u32x4* mine = reinterpret_cast<u32x4*>(smem + wid * 16384);
#pragma unroll
    for (int n = 0; n < 8; ++n)
      mine[n * 64 + lane] = u32x4{cvtpk(oacc[n][0][0], oacc[n][0][1]), cvtpk(oacc[n][0][2], oacc[n][0][3]),
                                  cvtpk(oacc[n][1][0], oacc[n][1][1]), cvtpk(oacc[n][1][2], oacc[n][1][3])};
    u32x2* mine2 = reinterpret_cast<u32x2*>(smem + wid * 16384 + 8192);
#pragma unroll
    for (int n = 0; n < 8; ++n)
      mine2[n * 64 + lane] = u32x2{cvtpk(oacc[n][2][0], oacc[n][2][1]), cvtpk(oacc[n][2][2], oacc[n][2][3])};
  }
  __syncthreads();
  u32x4 pa[NW]; u32x2 pb[NW];
#pragma unroll
  for (int w = 0; w < NW; ++w) {
    pa[w] = reinterpret_cast<const u32x4*>(smem + w * 16384)[wid * 64 + lane];
    pb[w] = reinterpret_cast<const u32x2*>(smem + w * 16384 + 8192)[wid * 64 + lane];
  }
  __hip_bfloat16* op = o + ((size_t)qhead * SQ + c) * DH + wid * 16 + g * 4;
#pragma unroll
  for (int t = 0; t < 3; ++t) {
    f32x2 s8[NW];
#pragma unroll
    for (int w = 0; w < NW; ++w) s8[w] = *reinterpret_cast<const f32x2*>(st + (w * SQ + t * 16 + c) * 2);
    float M = s8[0][0];
#pragma unroll
    for (int w = 1; w < NW; ++w) M = fmaxf(M, s8[w][0]);
    float L = 0.f;
    f32x4 a = {0.f, 0.f, 0.f, 0.f};
#pragma unroll
    for (int w = 0; w < NW; ++w) {
      const float f = __builtin_amdgcn_exp2f(s8[w][0] - M);
      L = fmaf(s8[w][1], f, L);
      const unsigned lo = t == 0 ? pa[w][0] : t == 1 ? pa[w][2] : pb[w][0];
      const unsigned hi = t == 0 ? pa[w][1] : t == 1 ? pa[w][3] : pb[w][1];
      a[0] = fmaf(__uint_as_float(lo << 16), f, a[0]);
      a[1] = fmaf(__uint_as_float(lo & 0xffff0000u), f, a[1]);
      a[2] = fmaf(__uint_as_float(hi << 16), f, a[2]);
      a[3] = fmaf(__uint_as_float(hi & 0xffff0000u), f, a[3]);
    }
    const float inv = __builtin_amdgcn_rcpf(L);
    u32x2 pk = {cvtpk(a[0] * inv, a[1] * inv), cvtpk(a[2] * inv, a[3] * inv)};
    *reinterpret_cast<u32x2*>(op + t * 16 * DH) = pk;
  }
}

extern "C" void kernel_launch(void* const* d_in, const int* in_sizes, int n_in,
                              void* d_out, int out_size, void* d_ws, size_t ws_size,
                              hipStream_t stream) {
  const __hip_bfloat16* q = (const __hip_bfloat16*)d_in[0];
  const __hip_bfloat16* k = (const __hip_bfloat16*)d_in[1];
  const __hip_bfloat16* v = (const __hip_bfloat16*)d_in[2];
  __hip_bfloat16* o = (__hip_bfloat16*)d_out;
  gqa_fwd_kernel<<<dim3(NB_ * HQ), dim3(NW * 64), 0, stream>>>(q, k, v, o
  );
}
```
